```python
import torch

def _softmax(x, axis=-1):
    return torch.softmax(x, dim=axis)

def _gelu(x):
    return torch.nn.functional.gelu(x, approximate='tanh')

def _silu(x):
    return torch.nn.functional.silu(x)

def _sigmoid(x):
    return torch.sigmoid(x)

def _layernorm(x, axis=-1, eps=1e-05):
    m = x.mean(dim=axis, keepdim=True)
    v = x.var(dim=axis, keepdim=True, unbiased=False)
    return (x - m) / torch.sqrt(v + eps)

def _scatter_add(tgt, idx, src, axis=0):
    return tgt.clone().index_add_(axis, idx, src)

def _segment_sum(src, seg, n):
    out = torch.zeros((n,) + src.shape[1:], dtype=src.dtype)
    return out.index_add_(0, seg, src)

def _pad(x, pw):
    flat = tuple((v for pair in reversed(pw) for v in pair))
    return torch.nn.functional.pad(x, flat)
kernel_name = 'gated_mix__memory_aug_recurrent_mem_tr__swiglu_ffn__prompt'

def setup_inputs():

    _w = lambda a: (lambda t: t.to(torch.bfloat16) if t.is_floating_point() else t)(torch.as_tensor(a))

    def _A_setup():
        B, S, Dm = (1, 2048, 448)
        H, Dh = (16, 256)
        M, seg = (8, 32)
        seg = min(seg, S)
        S = S // seg * seg
        return {'x': _w((lambda *s: torch.randn(*s, dtype=torch.bfloat16) * 0.05)(B, S, Dm)), 'mem0': _w((lambda *s: torch.randn(*s, dtype=torch.bfloat16) * 0.4)(B, M, Dm)), 'Wq': _w((lambda *s: torch.randn(*s, dtype=torch.bfloat16) * 6.4)(Dm, H, Dh) * Dm ** (-0.5)), 'Wk': _w((lambda *s: torch.randn(*s, dtype=torch.bfloat16) * 6.4)(Dm, H, Dh) * Dm ** (-0.5)), 'Wv': _w((lambda *s: torch.randn(*s, dtype=torch.bfloat16) * 0.4)(Dm, H, Dh) * Dm ** (-0.5)), 'Wo': _w((lambda *s: torch.randn(*s, dtype=torch.bfloat16) * 0.4)(H, Dh, Dm) * (H * Dh) ** (-0.5)), 'seg_len': seg}
    _A_in = _A_setup()

    def _B_setup():
        B, S, D, F = (1, 2048, 448, 1792)
        return {'x': _w((lambda *s: torch.randn(*s, dtype=torch.bfloat16) * 0.05)(B, S, D)), 'w_gate': _w((lambda *s: torch.randn(*s, dtype=torch.bfloat16) * 0.05)(D, F)), 'w_up': _w((lambda *s: torch.randn(*s, dtype=torch.bfloat16) * 0.05)(D, F)), 'w_down': _w((lambda *s: torch.randn(*s, dtype=torch.bfloat16) * 0.05)(F, D))}
    _B_in = _B_setup()
    return {'A_x': _A_in['x'], 'A_mem0': _A_in['mem0'], 'A_Wq': _A_in['Wq'], 'A_Wk': _A_in['Wk'], 'A_Wv': _A_in['Wv'], 'A_Wo': _A_in['Wo'], 'A_seg_len': _A_in['seg_len'], 'B_x': _B_in['x'], 'B_w_gate': _B_in['w_gate'], 'B_w_up': _B_in['w_up'], 'B_w_down': _B_in['w_down'], 'gate_w': _w((lambda *s: torch.randn(*s, dtype=torch.bfloat16) * 0.05)(448) * 16.0)}

def reference(A_x, A_mem0, A_Wq, A_Wk, A_Wv, A_Wo, A_seg_len, B_x, B_w_gate, B_w_up, B_w_down, gate_w):

    def _A_ref(x, mem0, Wq, Wk, Wv, Wo, seg_len):
        B, S, Dm = x.shape
        H, Dh = (Wq.shape[1], Wq.shape[2])
        M = mem0.shape[1]
        scale = Dh ** (-0.5)
        n_seg = S // seg_len
        L = M + seg_len
        idx = torch.arange(L)
        mask = (idx[:, None] < M) | (idx[:, None] >= idx[None, :])
        mem = mem0
        outs = []
        for s in range(n_seg):
            xs = x[:, s * seg_len:(s + 1) * seg_len, :].clone()
            h = torch.concatenate([mem, xs], axis=1)
            q = torch.einsum('btd,dhe->bhte', h, Wq)
            k = torch.einsum('btd,dhe->bhte', h, Wk)
            v = torch.einsum('btd,dhe->bhte', h, Wv)
            scores = torch.einsum('bhqe,bhke->bhqk', q, k) * scale
            scores = torch.where(mask[None, None], scores, -1000000000.0)
            p = _softmax(scores, axis=-1)
            o = torch.einsum('bhqk,bhke->bhqe', p, v)
            o = torch.einsum('bhte,hed->btd', o, Wo)
            mem = o[:, :M, :].clone()
            outs.append(o[:, M:, :])
        return torch.concatenate(outs, axis=1)

    def _B_ref(x, w_gate, w_up, w_down):
        g = _silu(x @ w_gate)
        u = x @ w_up
        return g * u @ w_down
    x = A_x
    g = _sigmoid((x * gate_w).sum(-1, keepdims=True))
    a = _A_ref(x, A_mem0, A_Wq, A_Wk, A_Wv, A_Wo, A_seg_len)
    b = _B_ref(x, B_w_gate, B_w_up, B_w_down)
    return g * a + (1 - g) * b
```

```cpp
#include <hip/hip_runtime.h>
#include <stdint.h>

#define DI __device__ __forceinline__
#define LAS __attribute__((address_space(3)))
#define GAS __attribute__((address_space(1)))
typedef unsigned short u16;
typedef __bf16 bf16x8 __attribute__((ext_vector_type(8)));
typedef short s16x4 __attribute__((ext_vector_type(4)));
typedef float f32x4 __attribute__((ext_vector_type(4)));
typedef float f32x16 __attribute__((ext_vector_type(16)));
typedef unsigned u32x4 __attribute__((ext_vector_type(4)));
typedef unsigned u32x2 __attribute__((ext_vector_type(2)));
typedef __amdgpu_buffer_rsrc_t rsrc_t;

constexpr int D = 448, NH = 16, NSEG = 64, FF = 1792;
constexpr size_t MB = 1u << 20;
constexpr size_t OFF_RING = 0;
constexpr size_t OFF_PROBE = 1 * MB;
constexpr size_t OFF_CTRL = 2 * MB;
constexpr size_t OFF_FLAG_ZU = OFF_CTRL + 4096;
constexpr size_t OFF_FLAG_H = OFF_FLAG_ZU + 64 * 256 * 4;
constexpr size_t OFF_FLAG_F = OFF_FLAG_H + 64 * 64 * 4;
constexpr size_t OFF_FLAG_P = OFF_FLAG_F + 64 * 32 * 4;
constexpr size_t CTRL_END = OFF_FLAG_P + 64 * 16 * 4;
constexpr size_t OFF_STAT = 2 * MB + 512 * 1024;
constexpr size_t SZ_MAT = (size_t)NH * D * D * 2;
constexpr size_t OFF_A = 3 * MB;
constexpr size_t OFF_AT = OFF_A + 7 * MB;
constexpr size_t OFF_BT = OFF_AT + 7 * MB;
constexpr size_t OFF_Z = OFF_BT + 7 * MB;
constexpr size_t OFF_U = OFF_Z + 28 * MB;
constexpr size_t OFF_ZM = OFF_U + 35 * MB;
constexpr size_t OFF_M = OFF_ZM + 7 * MB;
constexpr size_t OFF_H = OFF_M + 1 * MB;
constexpr size_t OFF_FO = OFF_H + 7 * MB;
constexpr size_t OFF_PX = OFF_FO + 4 * MB;
constexpr size_t OFF_PM = OFF_PX + 2 * MB;
constexpr size_t WS_END = OFF_PM + 1 * MB;
static_assert(WS_END + 2 * MB <= 256 * MB, "workspace");

constexpr int AUX_SC1 = 16;
constexpr unsigned SENT = 0xFFFFFFFFu;
constexpr float SC2 = 0.0625f * 1.4426950408889634f;

struct Params {
    const u16* x; const u16* mem0; const u16* Wq; const u16* Wk; const u16* Wv; const u16* Wo;
    const u16* Wg; const u16* Wu; const u16* Wd; const u16* gw;
    unsigned char* ws; u16* out;
    int tauM, tauS, tauX, pad;
};

DI unsigned pk2(float a, float b) { typedef __bf16 bf2 __attribute__((ext_vector_type(2))); bf2 v = {(__bf16)a, (__bf16)b}; return __builtin_bit_cast(unsigned, v); }
DI f32x4 mfma16(u32x4 a, u32x4 b, f32x4 c) { return __builtin_amdgcn_mfma_f32_16x16x32_bf16(__builtin_bit_cast(bf16x8, a), __builtin_bit_cast(bf16x8, b), c, 0, 0, 0); }
DI f32x4 mfma16k16(u32x2 a, u32x2 b, f32x4 c) { return __builtin_amdgcn_mfma_f32_16x16x16bf16_1k(__builtin_bit_cast(s16x4, a), __builtin_bit_cast(s16x4, b), c, 0, 0, 0); }
DI f32x16 mfma32(u32x4 a, u32x4 b, f32x16 c) { return __builtin_amdgcn_mfma_f32_32x32x16_bf16(__builtin_bit_cast(bf16x8, a), __builtin_bit_cast(bf16x8, b), c, 0, 0, 0); }
template <bool FIRST> DI void mma32_wa(f32x16& acc, const u32x4& w, const u32x4& x) {
    if (FIRST) asm volatile("v_mfma_f32_32x32x16_bf16 %0, %1, %2, 0" : "=&v"(acc) : "a"(w), "v"(x));
    else asm volatile("v_mfma_f32_32x32x16_bf16 %0, %1, %2, %0" : "+v"(acc) : "a"(w), "v"(x));
}
template <bool FIRST> DI void mma32_wb(f32x16& acc, const u32x4& x, const u32x4& w) {
    if (FIRST) asm volatile("v_mfma_f32_32x32x16_bf16 %0, %1, %2, 0" : "=&v"(acc) : "v"(x), "a"(w));
    else asm volatile("v_mfma_f32_32x32x16_bf16 %0, %1, %2, %0" : "+v"(acc) : "v"(x), "a"(w));
}
DI void mma32_done(f32x16& a, f32x16& b) { asm volatile("s_nop 7\n\ts_nop 3" : "+v"(a), "+v"(b)); }
DI u32x4 gld16(const void* p) { return *(const GAS u32x4*)p; }
DI u32x4 bld16(rsrc_t r, int voff, int soff) { return __builtin_amdgcn_raw_buffer_load_b128(r, voff, soff, AUX_SC1); }
DI u32x2 bld8(rsrc_t r, int voff, int soff) { return __builtin_amdgcn_raw_buffer_load_b64(r, voff, soff, AUX_SC1); }
DI unsigned bld4(rsrc_t r, int voff, int soff) { return __builtin_amdgcn_raw_buffer_load_b32(r, voff, soff, AUX_SC1); }
DI void bst16(u32x4 v, rsrc_t r, int voff, int soff) { __builtin_amdgcn_raw_buffer_store_b128(v, r, voff + soff, 0, AUX_SC1); }
DI void bst8(u32x2 v, rsrc_t r, int voff, int soff) { __builtin_amdgcn_raw_buffer_store_b64(v, r, voff, soff, AUX_SC1); }
DI void bst4(unsigned v, rsrc_t r, int voff, int soff) { __builtin_amdgcn_raw_buffer_store_b32(v, r, voff, soff, AUX_SC1); }
DI unsigned ct() { return (unsigned)__builtin_amdgcn_s_memtime(); }
DI void wait_until(unsigned t) { while ((int)(ct() - t) < 0) {} }
DI unsigned rt16() { return (unsigned)__builtin_amdgcn_s_memrealtime() << 4; }
DI void wait_slot(unsigned slot) { while ((int)(rt16() - slot) < 0) {} }
DI unsigned slot_max(const LAS unsigned char* w) {
    const u32x4 v = *(const LAS u32x4*)w;
    const unsigned a = (int)(v.x - v.y) > 0 ? v.x : v.y, b = (int)(v.z - v.w) > 0 ? v.z : v.w;
    return __builtin_amdgcn_readfirstlane((int)(a - b) > 0 ? a : b);
}
DI unsigned next_slot(unsigned slot, unsigned q) { const unsigned now = rt16(); do slot += q; while ((int)(now - slot) >= 0); return slot; }
template <int CTRL> DI float dpp(float v) { return __int_as_float(__builtin_amdgcn_update_dpp(0, __float_as_int(v), CTRL, 0xf, 0xf, true)); }
constexpr int ROR1 = 0x121, ROR2 = 0x122, ROR4 = 0x124, ROR8 = 0x128, QP0022 = 0xA0, QP1133 = 0xF5;
DI unsigned mx4(u32x4 v) { return max(max(v.x, v.y), max(v.z, v.w)); }
DI void lds_barrier() { asm volatile("s_waitcnt lgkmcnt(0)" ::: "memory"); __builtin_amdgcn_s_barrier(); asm volatile("" ::: "memory"); }
#define SCHED() __builtin_amdgcn_sched_barrier(0)
#define DRAIN() asm volatile("s_waitcnt vmcnt(0)" ::: "memory")
constexpr int SPIN_CAP = 1 << 18;
#define POLL_SLEEP 16
DI void note_timeout(rsrc_t rs, unsigned code) { bst4(code, rs, (int)OFF_CTRL + 64, 0); }

DI int pidx(int i, int r) { return 32 * (i >> 1) + 8 * (r >> 2) + 4 * (i & 1) + (r & 3); }

constexpr int NPREP = 512;
#define PREP_SC1 1
#define WARM 1
DI size_t piece(int h, int blk, int oct, int pos) { return ((((size_t)h * 14 + blk) * 56 + oct) * 32 + pos) * 16; }

__global__ void __launch_bounds__(256, 2) k_prep(Params p) {
    extern __shared__ __attribute__((aligned(16))) unsigned char smem[];
    const int tid = threadIdx.x, l = tid & 63, w = __builtin_amdgcn_readfirstlane(tid >> 6), r16 = l & 15, q = l >> 4;
    const int blk = blockIdx.x;
#define PSTAMP(x)
    {
        if (w == 0) {
            const int i = blk * 64 + l, fb = i >> 10, o = i & 511;
            u32x4 v = {SENT, SENT, SENT, SENT};
            if (fb < 2 && o < 448) { const int b = o >> 3, t = o & 7; v = gld16(p.mem0 + t * D + 8 * b); v.x = min(v.x, SENT - 1); v.y = min(v.y, SENT - 1); v.z = min(v.z, SENT - 1); v.w = min(v.w, SENT - 1); }
            *(GAS u32x4*)(p.ws + OFF_RING + (size_t)i * 16) = v;
        } else if (w & 1) {
            constexpr int NW = (int)((CTRL_END - OFF_CTRL) / 4);
            static_assert(NW <= 2 * NPREP * 64, "flags");
            const int i = (blk * 2 + (w >> 1)) * 64 + l;
            if (i < NW && (!(p.pad & 1) || i >= (int)((OFF_FLAG_F - OFF_CTRL) / 4) || i < 16)) ((GAS unsigned*)(p.ws + OFF_CTRL))[i] = 0;
        } else if (w == 2) {
            const int i = blk * 64 + l;
            u32x4 v = {SENT, SENT, SENT, SENT};
            if (i < 448) v = gld16(p.mem0 + (i & 7) * D + 8 * (i >> 3));
            if (i < 66 * 448) *(GAS u32x4*)(p.ws + OFF_M + (size_t)i * 16) = v;
        }
        {
            u32x4 t[7];
#pragma unroll
            for (int n = 0; n < 7; ++n) {
                const int idx = blk * 256 + tid + n * (NPREP * 256), m = idx / 229376, o = idx % 229376;
                const u16* base = m == 0 ? p.Wq : m == 1 ? p.Wk : m == 2 ? p.Wv : p.Wo;
                t[n] = gld16(base + (size_t)o * 8);
            }
#pragma unroll
            for (int n = 0; n < 7; ++n) asm volatile("" :: "v"(t[n]));
        }
        PSTAMP(t1);
        const int T = blk >> 3;
        const bool tA = T < 32;
        const int h = 2 * (blk & 7) + ((T >> 4) & 1), tr = (T >> 2) & 3, tc = T & 3;
        const int wr = w >> 1, wc = w & 1, rt = 2 * tr + wr, ctl = 2 * tc + wc;
        const bool active = rt < 7 && ctl < 7;
        LAS unsigned char* L = (LAS unsigned char*)smem;
        const char* baseR = (const char*)(tA ? p.Wq : p.Wv); const char* baseC = (const char*)(tA ? p.Wk : p.Wo);
        unsigned offR[4], offC[4];
#pragma unroll
        for (int n = 0; n < 4; ++n) {
            const int rho = 8 * (4 * w + n) + (l >> 3), ch = (l & 7) ^ (rho & 7);
            const int rr = min(128 * tr + 64 * (rho >> 6) + pidx((rho & 63) >> 4, rho & 15), D - 1);
            offR[n] = ((rr * 16 + h) * 256 + ch * 8) * 2;
            if (tA) { const int cc = min(128 * tc + 64 * (rho >> 6) + pidx((rho & 63) >> 4, rho & 15), D - 1); offC[n] = ((cc * 16 + h) * 256 + ch * 8) * 2; }
            else { const int e = 4 * (4 * w + n) + (l >> 4), c16 = (l & 15) ^ ((e & 7) << 1); offC[n] = ((h * 256 + e) * D + min(128 * tc + 8 * c16, D - 8)) * 2; }
        }
        auto dma = [&](int kc) {
            LAS unsigned char* b = L + (kc & 1) * 32768;
            const char* bR = baseR + 128 * kc; const char* bC = baseC + (tA ? 128 * kc : 128 * kc * D);
#pragma unroll
            for (int n = 0; n < 4; ++n) {
                __builtin_amdgcn_global_load_lds((const GAS unsigned*)(bR + offR[n]), (LAS unsigned*)(b + (4 * w + n) * 1024), 16, 0, 0);
                __builtin_amdgcn_global_load_lds((const GAS unsigned*)(bC + offC[n]), (LAS unsigned*)(b + 16384 + (4 * w + n) * 1024), 16, 0, 0);
            }
        };
        f32x4 a1[4][4], a2[4][4];
#pragma unroll
        for (int i = 0; i < 4; ++i)
#pragma unroll
            for (int j = 0; j < 4; ++j) { a1[i][j] = f32x4{0, 0, 0, 0}; a2[i][j] = a1[i][j]; }
        dma(0); dma(1);
#pragma unroll 1
        for (int kc = 0; kc < 4; ++kc) {
            if (kc < 3) asm volatile("s_waitcnt vmcnt(8)" ::: "memory"); else DRAIN();
            lds_barrier();
            const LAS unsigned char* b = L + (kc & 1) * 32768;
            if (active) {
#pragma unroll
                for (int k2 = 0; k2 < 2; ++k2) {
                    u32x4 fa[4], fb[4];
#pragma unroll
                    for (int i = 0; i < 4; ++i) { const int rho = 64 * wr + 16 * i + r16; fa[i] = *(const LAS u32x4*)(b + rho * 128 + ((4 * k2 + q) ^ (rho & 7)) * 16); }
                    if (tA) {
#pragma unroll
                        for (int j = 0; j < 4; ++j) { const int rho = 64 * wc + 16 * j + r16; fb[j] = *(const LAS u32x4*)(b + 16384 + rho * 128 + ((4 * k2 + q) ^ (rho & 7)) * 16); }
#pragma unroll
                        for (int i = 0; i < 4; ++i)
#pragma unroll
                            for (int j = 0; j < 4; ++j) { a1[i][j] = mfma16(fa[i], fb[j], a1[i][j]); a2[j][i] = mfma16(fb[j], fa[i], a2[j][i]); }
                    } else {
#pragma unroll
                        for (int j = 0; j < 4; ++j) {
                            const int e0 = 32 * k2 + 8 * q + (r16 >> 2), c16 = 8 * wc + 2 * j + ((l & 3) >> 1);
                            const s16x4 lo = __builtin_amdgcn_ds_read_tr16_b64_v4i16((LAS s16x4*)(b + 16384 + e0 * 256 + (c16 ^ ((e0 & 7) << 1)) * 16 + (l & 1) * 8));
                            const s16x4 hi = __builtin_amdgcn_ds_read_tr16_b64_v4i16((LAS s16x4*)(b + 16384 + (e0 + 4) * 256 + (c16 ^ (((e0 + 4) & 7) << 1)) * 16 + (l & 1) * 8));
                            const u32x2 x = __builtin_bit_cast(u32x2, lo), y = __builtin_bit_cast(u32x2, hi);
                            fb[j] = u32x4{x.x, x.y, y.x, y.y};
                        }
#pragma unroll
                        for (int i = 0; i < 4; ++i)
#pragma unroll
                            for (int j = 0; j < 4; ++j) a1[i][j] = mfma16(fa[i], fb[j], a1[i][j]);
                    }
                }
            }
            if (kc < 2) { lds_barrier(); dma(kc + 2); }
        }
        PSTAMP(t2);
        const rsrc_t rs = __builtin_amdgcn_make_buffer_rsrc(p.ws, 0, (int)WS_END, 0x00020000);
#define PST(v, off) bst16(v, rs, (int)(off), 0)
        if (active) {
#pragma unroll
            for (int P = 0; P < 2; ++P)
#pragma unroll
                for (int j = 0; j < 4; ++j) {
                    const u32x4 v1 = {pk2(a1[2 * P][j][0], a1[2 * P][j][1]), pk2(a1[2 * P][j][2], a1[2 * P][j][3]), pk2(a1[2 * P + 1][j][0], a1[2 * P + 1][j][1]), pk2(a1[2 * P + 1][j][2], a1[2 * P + 1][j][3])};
                    PST(v1, (tA ? OFF_AT : OFF_BT) + piece(h, 2 * ctl + (j >> 1), 8 * rt + 4 * P + q, 16 * (j & 1) + r16));
                    if (tA) {
                        const u32x4 v2 = {pk2(a2[2 * P][j][0], a2[2 * P][j][1]), pk2(a2[2 * P][j][2], a2[2 * P][j][3]), pk2(a2[2 * P + 1][j][0], a2[2 * P + 1][j][1]), pk2(a2[2 * P + 1][j][2], a2[2 * P + 1][j][3])};
                        PST(v2, OFF_A + piece(h, 2 * rt + (j >> 1), 8 * ctl + 4 * P + q, 16 * (j & 1) + r16));
                    }
                }
        }
    }
}

DI int s_phys(int o) { return (o >> 13) * 16384 + (o & 8191); }
DI int s_piece(int h, int pc, int m) { return s_phys((((m >> 2) * 4 + (h >> 2)) * 3 + (pc >> 2)) * 1024 + (pc & 3) * 256 + (h & 3) * 64 + (m & 3) * 16); }
constexpr int SLOT = 8 * 16384, MCOPY = 16384, SOFF = 2 * 16384, SCOPY = 3 * 16384;
constexpr int L_M = 0;
constexpr int L_ZERO = 7168;
constexpr int L_S = 8192;
constexpr int L_O = 9216;
constexpr int L_X = 10240;
constexpr int L_DUMMY = 14336;
constexpr int L_T = 15360;

DI bool bad2(u32x4 a, u32x4 b) { return __any(max(mx4(a), mx4(b)) == SENT); }

DI void probe_parity(const Params& p) {
    if (threadIdx.x >= 64) return;
    const int l = threadIdx.x;
    const rsrc_t rs = __builtin_amdgcn_make_buffer_rsrc(p.ws, 0, (int)WS_END, 0x00020000);
    unsigned xcc; asm volatile("s_getreg_b32 %0, hwreg(HW_REG_XCC_ID)" : "=s"(xcc));
    const int po = (int)OFF_PROBE + (l & 7) * 8192 + 512;
    bst4(0u, rs, po, 0);
    DRAIN();
    int best0 = 1 << 30, best1 = 1 << 30;
#pragma unroll 1
    for (int k = 0; k < 8; ++k) {
        const int o = __builtin_amdgcn_readlane(po, k);
        unsigned v; unsigned long long c0, c1;
        asm volatile("s_memtime %0\n\ts_waitcnt lgkmcnt(0)\n\tbuffer_load_dword %2, %3, %4, 0 offen sc1\n\ts_waitcnt vmcnt(0)\n\ts_memtime %1\n\ts_waitcnt lgkmcnt(0)"
                     : "=&s"(c0), "=&s"(c1), "=&v"(v) : "v"(o), "s"(rs) : "memory");
        const int dt = (int)(c1 - c0);
        if (k & 1) best1 = min(best1, dt); else best0 = min(best0, dt);
    }
    const unsigned mine = best1 < best0 ? 1u : 0u;
    const unsigned par = ((xcc & 15) >= 4) ? 1u - mine : mine;
    if (l == 0) bst4(2u | par, rs, (int)OFF_CTRL, 0);
}
DI int wait_parity(rsrc_t rs) {
    unsigned v = 0;
#pragma unroll 1
    for (int att = 0; att < SPIN_CAP; ++att) {
        asm volatile("" ::: "memory");
        v = bld4(rs, (int)OFF_CTRL, 0);
        if (v & 2u) break;
        __builtin_amdgcn_s_sleep(4);
    }
    return __builtin_amdgcn_readfirstlane(v) & 1;
}

#define VLD16(dst, voff, soff) asm volatile("buffer_load_dwordx4 %0, %1, %2, %3 offen sc1" : "=v"(dst) : "v"(voff), "s"(rs), "s"(soff) : "memory")
#define VLD4(dst, voff, soff) asm volatile("buffer_load_dword %0, %1, %2, %3 offen sc1" : "=v"(dst) : "v"(voff), "s"(rs), "s"(soff) : "memory")
#define VST4(val, voff, soff) asm volatile("buffer_store_dword %0, %1, %2, %3 offen sc1" :: "v"(val), "v"(voff), "s"(rs), "s"(soff) : "memory")
#define VST8(val, voff, soff) asm volatile("buffer_store_dwordx2 %0, %1, %2, %3 offen sc1" :: "v"(val), "v"(voff), "s"(rs), "s"(soff) : "memory")
#define MM_AV0(d, a, b) asm volatile("v_mfma_f32_16x16x32_bf16 %0, %1, %2, 0" : "=&v"(d) : "a"(a), "v"(b))
#define MM_AV(d, a, b) asm volatile("v_mfma_f32_16x16x32_bf16 %0, %1, %2, %0" : "+v"(d) : "a"(a), "v"(b))
#define MM_VA0(d, a, b) asm volatile("v_mfma_f32_16x16x32_bf16 %0, %1, %2, 0" : "=&v"(d) : "v"(a), "a"(b))
#define MM_VA(d, a, b) asm volatile("v_mfma_f32_16x16x32_bf16 %0, %1, %2, %0" : "+v"(d) : "v"(a), "a"(b))
constexpr int OOB = 0x7FFFFF00;
#define REPL 0
#define FFN_DELAY 1
#define MODE 0
#define ADAPT_UP 0
#define ADAPT_DN 0
#define XUP 64
#define XDN 16
#define RAMP 64
#define RAMPN 10
#define LPT(k, idx, base)
#define VWAIT() do { SCHED(); asm volatile("s_waitcnt vmcnt(0)" ::: "memory"); SCHED(); } while (0)
template <int V> struct IntC { static constexpr int value = V; };

DI void chain_merged(const Params& p, int role, LAS unsigned char* L) {
    const int tid = threadIdx.x, l = tid & 63, w = __builtin_amdgcn_readfirstlane(tid >> 6), r16 = l & 15, q = l >> 4;
    const int h = role / 7, c = role % 7, g = role / 56, j = role % 56;
    const int parl = (l >> 3) & 1;
    const rsrc_t rs = __builtin_amdgcn_make_buffer_rsrc(p.ws, 0, (int)WS_END, 0x00020000);
    u32x4 AT[4][4], AZ[14], BT[2][14];
    {
#pragma unroll
        for (int tt = 0; tt < 4; ++tt)
#pragma unroll
            for (int kk = 0; kk < 4; ++kk) {
                const int ks = min(4 * w + kk, 13);
                AT[tt][kk] = gld16(p.ws + OFF_AT + piece(h, 2 * c + (tt >> 1), 4 * ks + q, 16 * (tt & 1) + r16));
            }
#pragma unroll
        for (int u = 0; u < 2; ++u)
#pragma unroll
            for (int ks = 0; ks < 14; ++ks) BT[u][ks] = gld16(p.ws + OFF_BT + piece(4 * w + 2 * u + parl, j >> 2, 4 * ks + q, 8 * (j & 3) + (l & 7)));
        SCHED();
#pragma unroll
        for (int tt = 0; tt < 4; ++tt)
#pragma unroll
            for (int kk = 0; kk < 4; ++kk) {
                if (4 * w + kk > 13) AT[tt][kk] = u32x4{0, 0, 0, 0};
                asm volatile("" : "+a"(AT[tt][kk]));
            }
    }
    const unsigned ONE = 0x3F80u;
    u32x4 SEL0, SEL1; u32x2 AONE, AW; f32x4 CINIT;
    {
#pragma unroll
        for (int n = 0; n < 4; ++n) { const unsigned v = (r16 == 2 * n ? ONE : 0u) | (r16 == 2 * n + 1 ? ONE << 16 : 0u); SEL0[n] = v; SEL1[n] = q < 3 ? v : 0u; }
        if (q == 3 && r16 == 8) { SEL1[0] = ONE | (ONE << 16); SEL1[1] = ONE; }
        AONE = u32x2{ONE | (ONE << 16), ONE | (ONE << 16)};
        AW = u32x2{q == 2 ? ONE : 0u, 0u};
        const float NI = -3.0e38f;
        CINIT = q < 2 ? f32x4{0, 0, 0, 0} : (q == 2 ? f32x4{0, NI, NI, NI} : f32x4{NI, NI, NI, NI});
    }
    if (tid < 64) *(LAS u32x4*)(L + L_ZERO + tid * 16) = u32x4{0, 0, 0, 0};
    const int ring = (int)OFF_RING + wait_parity(rs) * 8192;
    const int lo_m = (4 * parl + q) * 128 + (l & 7) * 16;
    const int vm0 = ring + min(2 * w, 6) * 1024 + lo_m, vm1 = w < 3 ? ring + (2 * w + 1) * 1024 + lo_m : OOB;
    const int vs0 = ring + SOFF + s_phys(((g * 4 + w) * 3 + 0) * 1024 + l * 16), vs1 = ring + SOFF + s_phys(((g * 4 + w) * 3 + 1) * 1024 + l * 16), vs2 = ring + SOFF + s_phys(((g * 4 + w) * 3 + 2) * 1024 + l * 16);
    const int vspub = ring + SOFF + (l & 1) * SCOPY + s_piece(h, c, l >> 3) + ((l & 7) >> 1) * 4;
    const int m4 = (l >> 3) & 3, f8 = l & 7;
    const bool toring = (f8 & 1) == 0;
    const int vmring = ring + (l >> 5) * MCOPY + j * 128 + (4 * g + m4) * 16 + (f8 >> 1) * 4;
    const int vmglob = (int)OFF_M + j * 128 + (4 * g + m4) * 16 + (f8 >> 1) * 4;
    const int vra0 = vspub, vra1 = toring ? vmring : OOB;
    const int vu0 = (int)OFF_U + (((4 * w + parl) * 5 + q) * D + 8 * j + (l & 7)) * 16, vu1 = vu0 + 2 * 5 * D * 16;
    const int vy0 = l < 32 ? (int)OFF_U + (((4 * w + parl) * 5 + 4) * D + 8 * j + (l & 7)) * 16 + (q & 1) * 8 : OOB, vy1 = vy0 == OOB ? OOB : vy0 + 2 * 5 * D * 16;
    const int vz = parl ? OOB : (int)OFF_ZM + (h * 14 + 2 * c + (w >> 1)) * 512 + q * 128 + (l & 7) * 16 + (w & 1) * 8;
    const int vflag = (int)OFF_FLAG_ZU + (112 + ((4 * w + (l & 3)) * 14 + (j >> 2)) / 2) * 4;
    const LAS unsigned char* a0p = L + (parl ? L_ZERO : L_M + c * 1024) + l * 16;
    const LAS unsigned char* a1p = L + (parl ? L_M + c * 1024 : L_ZERO) + l * 16;
    const int hl = (l >> 2) & 3;
    const unsigned long long hvm = __builtin_amdgcn_ballot_w64((hl & 1) == (q >> 1)), hbm = __builtin_amdgcn_ballot_w64(hl >= 2), h1m = __builtin_amdgcn_ballot_w64((hl & 1) != 0);
    const unsigned SENTV = SENT;
    lds_barrier();
    unsigned tref = ct();
    unsigned slot = MODE == 1 ? (rt16() / p.tauX + 1) * p.tauX : 0;
    int tauM = p.tauM, tauS = p.tauS;
    u32x2 YA0 = {0, 0}, YA1 = {0, 0};
#define STAMP(i, from)
#define COUNT(i, v)
    const unsigned long long parm = __builtin_amdgcn_ballot_w64(parl != 0);
    LAS unsigned char* const ldm0 = L + L_M + (2 * w) * 1024 + l * 16;
    LAS unsigned char* const ldm1 = L + (w < 3 ? L_M + (2 * w + 1) * 1024 : L_DUMMY) + l * 16;
    const unsigned lds_sa = (unsigned)(size_t)(L + L_S + w * 256 + ((l & 7) * 8 + 4 * (q & 1) + 2 * parl + (l >> 5)) * 4);
    const unsigned lds_oa = (unsigned)(size_t)(L + L_O + w * 128 + ((l & 3) * 8 + 4 * (q & 1) + 2 * (hl & 1) + (l >> 5)) * 4);
    const unsigned a0a = (unsigned)(size_t)a0p, a1a = (unsigned)(size_t)a1p;
    const int mc0 = (REPL && ((role >> 2) & 1)) ? MCOPY : 0;
    u32x4 mf[7];
    const int nst = 2 + (g == 0 ? 2 : 0) + (w == 1 ? 2 : 0);
    u32x2 ZS = {0, 0};
    auto tail = [&]() {
        f32x4 ze, zo;
        asm volatile("v_mfma_f32_16x16x32_bf16 %0, %2, %4, 0\n\tv_mfma_f32_16x16x32_bf16 %1, %3, %4, 0" : "=&v"(ze), "=&v"(zo) : "a"(AZ[0]), "a"(AZ[1]), "v"(mf[0]));
#pragma unroll
        for (int i = 1; i < 7; ++i)
            asm volatile("v_mfma_f32_16x16x32_bf16 %0, %2, %4, %0\n\tv_mfma_f32_16x16x32_bf16 %1, %3, %4, %1" : "+v"(ze), "+v"(zo) : "a"(AZ[2 * i]), "a"(AZ[2 * i + 1]), "v"(mf[i]));
        asm volatile("s_nop 7" : "+v"(ze), "+v"(zo));
        float z[4];
#pragma unroll
        for (int r = 0; r < 4; ++r) z[r] = ze[r] + dpp<ROR8>(zo[r]);
        ZS = u32x2{pk2(z[0], z[1]), pk2(z[2], z[3])};
        asm volatile("" : "+v"(ZS));
    };
    auto stores = [&](int t) {
        const int vz_ = vz, vra0_ = vra0, vra1_ = vra1, vy0_ = vy0, vy1_ = vy1; const unsigned sent_ = SENTV;
        VST8(ZS, vz_, t * (16 * 14 * 512));
        if (g == 0) { VST8(YA0, vy0_, t * (16 * 5 * D * 16)); VST8(YA1, vy1_, t * (16 * 5 * D * 16)); }
        if (w == 1) { const int sp = ((t - 1) & 3) * SLOT; VST4(sent_, vra0_, sp); VST4(sent_, vra1_, sp); }
    };
    auto step = [&](int s, auto kindc) -> bool {
        constexpr bool LAST = decltype(kindc)::value == 2, FIRST = decltype(kindc)::value == 0;
        const int so = (s & 3) * SLOT, sU = s * (16 * 5 * D * 16);
        if (MODE == 1) wait_slot(slot); else if (!FIRST) wait_until(tref + tauM);
        STAMP(0, tref);
        u32x4 m0, m1;
        LPT(5, 0, slotA);
        VLD16(m0, vm0, so + mc0); VLD16(m1, vm1, so + mc0);
        VWAIT();
        LPT(2, 0, slotA);
        f32x4 te0, to0, te1, to1, te2, to2, te3, to3;
        unsigned chk;
#define T_HEAD() asm volatile( \
            "v_mfma_f32_16x16x32_bf16 %0, %5, %13, 0\n\tv_mfma_f32_16x16x32_bf16 %1, %6, %13, 0\n\t" \
            "v_max3_u32 %4, %15, %16, %17\n\t" \
            "v_mfma_f32_16x16x32_bf16 %2, %9, %13, 0\n\t" \
            "v_max3_u32 %4, %4, %18, %19\n\t" \
            "v_mfma_f32_16x16x32_bf16 %3, %10, %13, 0\n\t" \
            "v_max3_u32 %4, %4, %20, %21\n\t" \
            "v_mfma_f32_16x16x32_bf16 %0, %7, %14, %0\n\t" \
            "v_max_u32 %4, %4, %22\n\t" \
            "v_mfma_f32_16x16x32_bf16 %1, %8, %14, %1\n\tv_mfma_f32_16x16x32_bf16 %2, %11, %14, %2\n\tv_mfma_f32_16x16x32_bf16 %3, %12, %14, %3" \
            : "=&v"(te0), "=&v"(to0), "=&v"(te1), "=&v"(to1), "=&v"(chk) \
            : "a"(AT[0][0]), "a"(AT[0][1]), "a"(AT[0][2]), "a"(AT[0][3]), "a"(AT[1][0]), "a"(AT[1][1]), "a"(AT[1][2]), "a"(AT[1][3]), "v"(m0), "v"(m1), \
              "v"(m0.x), "v"(m0.y), "v"(m0.z), "v"(m0.w), "v"(m1.x), "v"(m1.y), "v"(m1.z), "v"(m1.w))
        T_HEAD();
        COUNT(8, 1);
        if (__any(chk == SENT)) {
#pragma unroll 1
            for (int att = 0;; ++att) {
                COUNT(8, 1);
                if (MODE == 1) { slot = next_slot(slot, p.tauX); wait_slot(slot); }
                asm volatile("buffer_load_dwordx4 %0, %2, %4, %5 offen sc1\n\tbuffer_load_dwordx4 %1, %3, %4, %5 offen sc1\n\ts_waitcnt vmcnt(0)" : "=&v"(m0), "=&v"(m1) : "v"(vm0), "v"(vm1), "s"(rs), "s"(so + MCOPY - mc0) : "memory");
                if (!bad2(m0, m1)) break;
                if (att > SPIN_CAP) { note_timeout(rs, 0x100 + s); return false; }
            }
            T_HEAD();
            if (MODE == 0) tauM += ADAPT_UP;
        }
        if (MODE == 0) tauM -= ADAPT_DN;
        slot += p.tauS + max(0, RAMPN - s) * RAMP;
        if (FIRST) {
#pragma unroll
            for (int ks = 0; ks < 14; ++ks) AZ[ks] = gld16(p.ws + OFF_A + piece(h, 2 * c + (w >> 1), 4 * ks + q, 16 * (w & 1) + r16));
            SCHED();
        }
        STAMP(1, ta);
        *(LAS u32x4*)ldm0 = m0;
        *(LAS u32x4*)ldm1 = m1;
        u32x4 B0;
        {
            unsigned c0, c1;
            asm volatile(
                "v_mfma_f32_16x16x32_bf16 %0, %[a20], %[m0], 0\n\tv_mfma_f32_16x16x32_bf16 %1, %[a21], %[m0], 0\n\t"
                "v_mfma_f32_16x16x32_bf16 %2, %[a30], %[m0], 0\n\tv_mfma_f32_16x16x32_bf16 %3, %[a31], %[m0], 0\n\t"
                "v_cndmask_b32 %8, %[e00], %[o00], %[pm]\n\tv_cndmask_b32 %9, %[e01], %[o01], %[pm]\n\t"
                "v_mfma_f32_16x16x32_bf16 %0, %[a22], %[m1], %0\n\t"
                "v_cvt_pk_bf16_f32 %4, %8, %9\n\tv_cndmask_b32 %8, %[e02], %[o02], %[pm]\n\tv_cndmask_b32 %9, %[e03], %[o03], %[pm]\n\t"
                "v_mfma_f32_16x16x32_bf16 %1, %[a23], %[m1], %1\n\t"
                "v_cvt_pk_bf16_f32 %5, %8, %9\n\tv_cndmask_b32 %8, %[e10], %[o10], %[pm]\n\tv_cndmask_b32 %9, %[e11], %[o11], %[pm]\n\t"
                "v_mfma_f32_16x16x32_bf16 %2, %[a32], %[m1], %2\n\t"
                "v_cvt_pk_bf16_f32 %6, %8, %9\n\tv_cndmask_b32 %8, %[e12], %[o12], %[pm]\n\tv_cndmask_b32 %9, %[e13], %[o13], %[pm]\n\t"
                "v_mfma_f32_16x16x32_bf16 %3, %[a33], %[m1], %3\n\t"
                "v_cvt_pk_bf16_f32 %7, %8, %9"
                : "=&v"(te2), "=&v"(to2), "=&v"(te3), "=&v"(to3), "=&v"(B0.x), "=&v"(B0.y), "=&v"(B0.z), "=&v"(B0.w), "=&v"(c0), "=&v"(c1)
                : [a20] "a"(AT[2][0]), [a21] "a"(AT[2][1]), [a22] "a"(AT[2][2]), [a23] "a"(AT[2][3]), [a30] "a"(AT[3][0]), [a31] "a"(AT[3][1]), [a32] "a"(AT[3][2]), [a33] "a"(AT[3][3]),
                  [m0] "v"(m0), [m1] "v"(m1), [pm] "s"(parm),
                  [e00] "v"(te0[0]), [e01] "v"(te0[1]), [e02] "v"(te0[2]), [e03] "v"(te0[3]), [o00] "v"(to0[0]), [o01] "v"(to0[1]), [o02] "v"(to0[2]), [o03] "v"(to0[3]),
                  [e10] "v"(te1[0]), [e11] "v"(te1[1]), [e12] "v"(te1[2]), [e13] "v"(te1[3]), [o10] "v"(to1[0]), [o11] "v"(to1[1]), [o12] "v"(to1[2]), [o13] "v"(to1[3]));
        }
        lds_barrier();
        LPT(3, 0, slotA);
        unsigned t2 = 0;
        if (!LAST) {
            asm volatile(
                "ds_read_b128 v[232:235], %[a0]\n\tds_read_b128 v[236:239], %[a1]\n\t"
                "s_nop 1\n\t"
                "v_cndmask_b32 v224, %[e20], %[o20], %[pm]\n\tv_cndmask_b32 v225, %[e21], %[o21], %[pm]\n\tv_cvt_pk_bf16_f32 v228, v224, v225\n\t"
                "v_cndmask_b32 v224, %[e22], %[o22], %[pm]\n\tv_cndmask_b32 v225, %[e23], %[o23], %[pm]\n\tv_cvt_pk_bf16_f32 v229, v224, v225\n\t"
                "v_cndmask_b32 v224, %[e30], %[o30], %[pm]\n\tv_cndmask_b32 v225, %[e31], %[o31], %[pm]\n\tv_cvt_pk_bf16_f32 v230, v224, v225\n\t"
                "v_cndmask_b32 v224, %[e32], %[o32], %[pm]\n\tv_cndmask_b32 v225, %[e33], %[o33], %[pm]\n\tv_cvt_pk_bf16_f32 v231, v224, v225\n\t"
                "s_waitcnt lgkmcnt(0)\n\t"
                "v_mfma_f32_16x16x32_bf16 v[240:243], v[232:235], %[b0], 0\n\t"
                "v_mfma_f32_16x16x32_bf16 v[240:243], v[236:239], v[228:231], v[240:243]\n\t"
                "s_nop 7\n\t"
                "v_add_f32_dpp v240, v240, v240 row_ror:8 row_mask:0xf bank_mask:0xf\n\tv_add_f32_dpp v241, v241, v241 row_ror:8 row_mask:0xf bank_mask:0xf\n\t"
                "v_add_f32_dpp v242, v242, v242 row_ror:8 row_mask:0xf bank_mask:0xf\n\tv_add_f32_dpp v243, v243, v243 row_ror:8 row_mask:0xf bank_mask:0xf\n\t"
                "v_permlane32_swap_b32 v240, v241\n\ts_nop 0\n\tv_permlane32_swap_b32 v242, v243\n\t"
                "v_add_f32 v240, v240, v241\n\tv_add_f32 v242, v242, v243\n\tv_cndmask_b32 v240, v240, v242, %[pm]\n\t"
                "ds_write_b32 %[dst], v240"
                :: [a0] "v"(a0a), [a1] "v"(a1a), [b0] "v"(B0), [pm] "s"(parm), [dst] "v"(lds_sa),
                   [e20] "v"(te2[0]), [e21] "v"(te2[1]), [e22] "v"(te2[2]), [e23] "v"(te2[3]), [o20] "v"(to2[0]), [o21] "v"(to2[1]), [o22] "v"(to2[2]), [o23] "v"(to2[3]),
                   [e30] "v"(te3[0]), [e31] "v"(te3[1]), [e32] "v"(te3[2]), [e33] "v"(te3[3]), [o30] "v"(to3[0]), [o31] "v"(to3[1]), [o32] "v"(to3[2]), [o33] "v"(to3[3])
                : "memory", "v224", "v225", "v228", "v229", "v230", "v231", "v232", "v233", "v234", "v235", "v236", "v237", "v238", "v239", "v240", "v241", "v242", "v243");
            if (MODE == 1) *(LAS unsigned*)(L + L_T + w * 4) = slot;
            lds_barrier();
            LPT(4, 0, slotA);
            t2 = ct();
            STAMP(2, ta);
            if (w == 0) {
                const LAS float* sp = (const LAS float*)(L + L_S) + l;
                const float v = (sp[0] + sp[64]) + (sp[128] + sp[192]);
                const unsigned pv = min(pk2(dpp<QP0022>(v), dpp<QP1133>(v)), SENT - 1);
                VST4(pv, vspub, so);
                LPT(1, 0, slotA);
            }
        }
        SCHED();
        if (MODE == 1 && !LAST) slot = slot_max(L + L_T);
        u32x4 U0, U1; unsigned flg;
        f32x4 ye0, yo0, ye1, yo1;
#pragma unroll
        for (int i = 0; i < 7; ++i) mf[i] = *(const LAS u32x4*)(L + L_M + i * 1024 + l * 16);
#define Y_STEP0(i) asm volatile("v_mfma_f32_16x16x32_bf16 %0, %4, %5, 0\n\tv_mfma_f32_16x16x32_bf16 %1, %4, %6, 0\n\tv_mfma_f32_16x16x32_bf16 %2, %4, %7, 0\n\tv_mfma_f32_16x16x32_bf16 %3, %4, %8, 0" \
            : "=&v"(ye0), "=&v"(yo0), "=&v"(ye1), "=&v"(yo1) : "v"(mf[i]), "a"(BT[0][2 * i]), "a"(BT[0][2 * i + 1]), "a"(BT[1][2 * i]), "a"(BT[1][2 * i + 1]))
#define Y_STEP(i) asm volatile("v_mfma_f32_16x16x32_bf16 %0, %4, %5, %0\n\tv_mfma_f32_16x16x32_bf16 %1, %4, %6, %1\n\tv_mfma_f32_16x16x32_bf16 %2, %4, %7, %2\n\tv_mfma_f32_16x16x32_bf16 %3, %4, %8, %3" \
            : "+v"(ye0), "+v"(yo0), "+v"(ye1), "+v"(yo1) : "v"(mf[i]), "a"(BT[0][2 * i]), "a"(BT[0][2 * i + 1]), "a"(BT[1][2 * i]), "a"(BT[1][2 * i + 1]))
        Y_STEP0(0); Y_STEP(1);
        SCHED();
        if (FIRST) {
#pragma unroll 1
            for (int att = 0;; ++att) {
                unsigned f2;
                asm volatile("buffer_load_dword %0, %1, %2, 0 offen sc1\n\ts_waitcnt vmcnt(0)" : "=&v"(f2) : "v"(vflag), "s"(rs) : "memory");
                if (!__any(f2 == 0)) break;
                if (att > SPIN_CAP) { note_timeout(rs, 0x200); return false; }
                __builtin_amdgcn_s_sleep(2);
            }
        }
        if (!LAST) { VLD16(U0, vu0, sU); VLD16(U1, vu1, sU); VLD4(flg, vflag, (s + 1) * 1024); }
        SCHED();
        Y_STEP(2);
        SCHED();
        u32x4 s0, s1, s2;
        if (!LAST) {
            STAMP(3, ta);
            if (MODE == 1) wait_slot(slot); else wait_until(t2 + tauS);
            STAMP(4, ta);
            LPT(5, 1, slotB);
            VLD16(s0, vs0, so); VLD16(s1, vs1, so); VLD16(s2, vs2, so);
        }
        SCHED();
        Y_STEP(3); Y_STEP(4); Y_STEP(5); Y_STEP(6);
        asm volatile("s_nop 7" : "+v"(ye0), "+v"(yo0), "+v"(ye1), "+v"(yo1));
        {
            float y[2][4];
#pragma unroll
            for (int r = 0; r < 4; ++r) {
                const auto x = __builtin_amdgcn_permlane32_swap(__float_as_uint(yo0[r]), __float_as_uint(yo0[r]), false, false); y[0][r] = ye0[r] + __uint_as_float(x[1]);
                const auto z = __builtin_amdgcn_permlane32_swap(__float_as_uint(yo1[r]), __float_as_uint(yo1[r]), false, false); y[1][r] = ye1[r] + __uint_as_float(z[1]);
            }
            YA0 = u32x2{pk2(y[0][0], y[0][1]), pk2(y[0][2], y[0][3])}; YA1 = u32x2{pk2(y[1][0], y[1][1]), pk2(y[1][2], y[1][3])};
            asm volatile("" : "+v"(YA0), "+v"(YA1));
        }
        if (LAST) return true;
        u32x2 YM0 = l < 32 ? YA0 : u32x2{0, 0}, YM1 = l < 32 ? YA1 : u32x2{0, 0};
        const int vmp = toring ? vmring + ((s + 1) & 3) * SLOT : vmglob + (s + 1) * 7168;
        asm volatile("" : "+v"(YM0), "+v"(YM1));
        SCHED();
        STAMP(5, ta);
        LPT(3, 1, slotB);
        VWAIT();
        LPT(2, 1, slotB);
        f32x4 dxa, dxb, ds;
#define B_HEAD() asm volatile( \
            "v_mfma_f32_16x16x32_bf16 %0, %4, %6, %10\n\tv_mfma_f32_16x16x32_bf16 %0, %5, %7, %0\n\t" \
            "v_max3_u32 %3, %12, %13, %14\n\tv_max3_u32 %3, %3, %15, %16\n\t" \
            "v_mfma_f32_16x16x32_bf16 %1, %8, %11, 0\n\t" \
            "v_max3_u32 %3, %3, %17, %18\n\tv_max3_u32 %3, %3, %19, %20\n\t" \
            "v_mfma_f32_16x16x32_bf16 %2, %9, %11, 0\n\t" \
            "v_max3_u32 %3, %3, %21, %22\n\tv_max_u32 %3, %3, %23" \
            : "=&v"(ds), "=&v"(dxa), "=&v"(dxb), "=&v"(chk) \
            : "v"(SEL0), "v"(SEL1), "v"(s0), "v"(s1), "v"(U0), "v"(U1), "v"(CINIT), "v"(s2), \
              "v"(s0.x), "v"(s0.y), "v"(s0.z), "v"(s0.w), "v"(s1.x), "v"(s1.y), "v"(s1.z), "v"(s1.w), "v"(s2.x), "v"(s2.y), "v"(s2.z), "v"(s2.w))
        B_HEAD();
        COUNT(9, 1);
        if (__any(chk == SENT)) {
#pragma unroll 1
            for (int att = 0;; ++att) {
                COUNT(9, 1);
                if (MODE == 1) { slot = next_slot(slot, p.tauX); wait_slot(slot); }
                asm volatile("buffer_load_dwordx4 %0, %3, %6, %7 offen sc1\n\tbuffer_load_dwordx4 %1, %4, %6, %7 offen sc1\n\tbuffer_load_dwordx4 %2, %5, %6, %7 offen sc1\n\ts_waitcnt vmcnt(0)"
                             : "=&v"(s0), "=&v"(s1), "=&v"(s2) : "v"(vs0), "v"(vs1), "v"(vs2), "s"(rs), "s"(so + SCOPY) : "memory");
                if (!__any(max(max(mx4(s0), mx4(s1)), mx4(s2)) == SENT)) break;
                if (att > SPIN_CAP) { note_timeout(rs, 0x300 + s); return false; }
            }
            B_HEAD();
            if (MODE == 0) tauS += ADAPT_UP;
        }
        if (MODE == 0) tauS -= ADAPT_DN;
        slot += p.tauM + max(0, RAMPN - s) * RAMP;
        STAMP(6, ta);
        asm volatile(
            "v_max3_f32 v224, %[d0], %[d1], %[d2]\n\t"
            "v_max_f32 v224, v224, %[d3]\n\t"
            "v_mov_b32 v225, v224\n\t"
            "s_nop 1\n\t"
            "v_permlane32_swap_b32 v224, v225\n\t"
            "v_max_f32 v224, v224, v225\n\t"
            "v_mov_b32 v225, v224\n\t"
            "s_nop 1\n\t"
            "v_permlane16_swap_b32 v224, v225\n\t"
            "v_max_f32 v224, v224, v225\n\t"
            "v_mul_f32 v224, 0xbdb8aa3b, v224\n\t"
            "v_fmamk_f32 v226, %[d0], 0x3db8aa3b, v224\n\t"
            "v_fmamk_f32 v227, %[d1], 0x3db8aa3b, v224\n\t"
            "v_exp_f32 v226, v226\n\t"
            "v_fmamk_f32 v228, %[d2], 0x3db8aa3b, v224\n\t"
            "v_exp_f32 v227, v227\n\t"
            "v_fmamk_f32 v229, %[d3], 0x3db8aa3b, v224\n\t"
            "v_exp_f32 v228, v228\n\t"
            "v_exp_f32 v229, v229\n\t"
            "v_cvt_pk_bf16_f32 v230, v226, v227\n\t"
            "v_cvt_pk_bf16_f32 v231, v228, v229\n\t"
            "v_cndmask_b32 v224, %[xa0], %[xb0], %[hb]\n\tv_cndmask_b32 v225, %[xa1], %[xb1], %[hb]\n\t"
            "v_mfma_f32_16x16x16_bf16 v[232:235], %[one], v[230:231], 0\n\t"
            "v_mfma_f32_16x16x16_bf16 v[236:239], %[aw], v[230:231], 0\n\t"
            "v_mfma_f32_16x16x16_bf16 v[240:243], %[y0], v[230:231], 0\n\t"
            "v_mfma_f32_16x16x16_bf16 v[244:247], %[y1], v[230:231], 0\n\t"
            "v_cndmask_b32 v226, %[xa2], %[xb2], %[hb]\n\tv_cndmask_b32 v227, %[xa3], %[xb3], %[hb]\n\t"
            "s_nop 2\n\t"
            "v_rcp_f32 v228, v232\n\t"
            "s_nop 0\n\t"
            "v_cndmask_b32 v228, 0, v228, %[hv]\n\t"
            "v_mul_f32 v229, v236, v228\n\t"
            "v_cndmask_b32 v240, v240, v244, %[hb]\n\tv_cndmask_b32 v241, v241, v245, %[hb]\n\t"
            "v_cndmask_b32 v242, v242, v246, %[hb]\n\tv_cndmask_b32 v243, v243, v247, %[hb]\n\t"
            "v_mul_f32 v240, v240, v228\n\tv_mul_f32 v241, v241, v228\n\tv_mul_f32 v242, v242, v228\n\tv_mul_f32 v243, v243, v228\n\t"
            "v_fmac_f32 v240, v224, v229\n\tv_fmac_f32 v241, v225, v229\n\tv_fmac_f32 v242, v226, v229\n\tv_fmac_f32 v243, v227, v229\n\t"
            "v_add_f32_dpp v240, v240, v240 row_ror:4 row_mask:0xf bank_mask:0xf\n\tv_add_f32_dpp v241, v241, v241 row_ror:4 row_mask:0xf bank_mask:0xf\n\t"
            "v_add_f32_dpp v242, v242, v242 row_ror:4 row_mask:0xf bank_mask:0xf\n\tv_add_f32_dpp v243, v243, v243 row_ror:4 row_mask:0xf bank_mask:0xf\n\t"
            "v_add_f32_dpp v240, v240, v240 row_ror:8 row_mask:0xf bank_mask:0xf\n\tv_add_f32_dpp v241, v241, v241 row_ror:8 row_mask:0xf bank_mask:0xf\n\t"
            "v_add_f32_dpp v242, v242, v242 row_ror:8 row_mask:0xf bank_mask:0xf\n\tv_add_f32_dpp v243, v243, v243 row_ror:8 row_mask:0xf bank_mask:0xf\n\t"
            "v_permlane32_swap_b32 v240, v241\n\ts_nop 0\n\tv_permlane32_swap_b32 v242, v243\n\t"
            "v_add_f32 v240, v240, v241\n\tv_add_f32 v242, v242, v243\n\tv_cndmask_b32 v240, v240, v242, %[h1]\n\t"
            "ds_write_b32 %[dst], v240"
            :: [d0] "v"(ds[0]), [d1] "v"(ds[1]), [d2] "v"(ds[2]), [d3] "v"(ds[3]), [one] "v"(AONE), [aw] "v"(AW), [y0] "v"(YM0), [y1] "v"(YM1),
               [hb] "s"(hbm), [hv] "s"(hvm), [h1] "s"(h1m), [dst] "v"(lds_oa),
               [xa0] "v"(dxa[0]), [xa1] "v"(dxa[1]), [xa2] "v"(dxa[2]), [xa3] "v"(dxa[3]), [xb0] "v"(dxb[0]), [xb1] "v"(dxb[1]), [xb2] "v"(dxb[2]), [xb3] "v"(dxb[3])
            : "memory", "v224", "v225", "v226", "v227", "v228", "v229", "v230", "v231", "v232", "v233", "v234", "v235", "v236", "v237", "v238", "v239",
              "v240", "v241", "v242", "v243", "v244", "v245", "v246", "v247");
        if (MODE == 1) *(LAS unsigned*)(L + L_T + 16 + w * 4) = slot;
        lds_barrier();
        LPT(4, 1, slotB);
        tref = ct();
        STAMP(7, ta);
        if (w == 0) {
            const LAS float* op = (const LAS float*)(L + L_O) + (l & 31);
            const float v = (op[0] + op[32]) + (op[64] + op[96]);
            const unsigned pv = min(pk2(dpp<QP0022>(v), dpp<QP1133>(v)), SENT - 1);
            VST4(pv, vmp, 0);
            LPT(1, 1, slotB);
        }
        SCHED();
        if (MODE == 1) slot = slot_max(L + L_T + 16);
        tail();
        SCHED();
        stores(s);
        if (__any(flg == 0)) {
#pragma unroll 1
            for (int att = 0;; ++att) {
                unsigned f2;
                asm volatile("buffer_load_dword %0, %1, %2, %3 offen sc1\n\ts_waitcnt vmcnt(0)" : "=&v"(f2) : "v"(vflag), "s"(rs), "s"((s + 1) * 1024) : "memory");
                if (!__any(f2 == 0)) break;
                if (att > SPIN_CAP) { note_timeout(rs, 0x200 + s); return false; }
                __builtin_amdgcn_s_sleep(2);
            }
        }
        return true;
    };
    if (!step(0, IntC<0>{})) return;
#pragma unroll
    for (int ks = 0; ks < 14; ++ks) asm volatile("" : "+a"(AZ[ks]));
#pragma unroll 1
    for (int s = 1; s < NSEG - 1; ++s) if (!step(s, IntC<1>{})) return;
    if (!step(NSEG - 1, IntC<2>{})) return;
    tail();
    DRAIN();
    stores(NSEG - 1);
    lds_barrier();
    if (w == 0) { const unsigned z = 0; VST4(z, toring ? OOB : vmglob, NSEG * 7168); }
    DRAIN();
    lds_barrier();
    if (w == 0) { const unsigned z = 0; VST4(z, toring ? OOB : vmglob, (NSEG + 1) * 7168); }
}

DI void chain_mx(const Params& p, int h, LAS unsigned char* L) {
    const int tid = threadIdx.x, l = tid & 63, w = __builtin_amdgcn_readfirstlane(tid >> 6), q = l >> 4;
    const int parl = (l >> 3) & 1;
    const rsrc_t rs = __builtin_amdgcn_make_buffer_rsrc(p.ws, 0, (int)WS_END, 0x00020000);
    const int ring = (int)OFF_RING + wait_parity(rs) * 8192;
    const int lo_m = (4 * parl + q) * 128 + (l & 7) * 16;
    const int vm0 = ring + min(2 * w, 6) * 1024 + lo_m, vm1 = w < 3 ? ring + (2 * w + 1) * 1024 + lo_m : OOB;
    int vz[2][4];
#pragma unroll
    for (int kt = 0; kt < 2; ++kt)
#pragma unroll
        for (int kk = 0; kk < 4; ++kk) vz[kt][kk] = (int)OFF_Z + h * 28 * 1024 + l * 16 + (kt * 14 + min(4 * w + kk, 13)) * 1024;
    const int vflag = (int)OFF_FLAG_ZU + (h * 7 + min(l & 7, 6)) * 4;
    const int tok = 2 * w + (l >> 5), key = l & 31;
    const int vppub = ring + SOFF + (l & 1) * SCOPY + s_piece(h, 8 + (key >> 3), tok) + ((key & 7) >> 1) * 4;
    const int vlpub = key < 8 ? ring + SOFF + ((key >> 2) & 1) * SCOPY + s_piece(h, 7, tok) + (key & 3) * 4 : OOB;
    const unsigned long long parm = __builtin_amdgcn_ballot_w64(parl != 0), k0m = __builtin_amdgcn_ballot_w64((key & 3) == 0), k1m = __builtin_amdgcn_ballot_w64((key & 3) == 1);
    LAS unsigned char* const lds_w = L + L_X + w * 1024 + ((l & 7) * 32 + 16 * parl + 4 * q) * 4;
    const LAS float* const xp = (const LAS float*)(L + L_X) + tok * 32 + key;
    const unsigned SENTV = SENT;
    int tau = 0;
    unsigned slot = 0;
#define XSTAMP(i)
    int natt = 0;
    u32x4 Z00, Z01, Z02, Z03, Z10, Z11, Z12, Z13;
#define LOAD_Z(sz) do { VLD16(Z00, vz[0][0], sz); VLD16(Z01, vz[0][1], sz); VLD16(Z02, vz[0][2], sz); VLD16(Z03, vz[0][3], sz); \
                        VLD16(Z10, vz[1][0], sz); VLD16(Z11, vz[1][1], sz); VLD16(Z12, vz[1][2], sz); VLD16(Z13, vz[1][3], sz); } while (0)
#define WAIT_FLAG(sg) _Pragma("unroll 1") for (int att = 0;; ++att) { unsigned f2; asm volatile("buffer_load_dword %0, %1, %2, %3 offen sc1\n\ts_waitcnt vmcnt(0)" : "=&v"(f2) : "v"(vflag), "s"(rs), "s"((sg) * 1024) : "memory"); \
        if (!__any(f2 == 0)) break; if (att > SPIN_CAP) { note_timeout(rs, 0x400 + (sg)); return; } __builtin_amdgcn_s_sleep(2); }
    unsigned flg;
    WAIT_FLAG(0);
    LOAD_Z(0);
    VLD4(flg, vflag, 1024);
    if (MODE == 1) slot = (rt16() / p.tauX + 1) * p.tauX;
    tau = p.tauX;
    unsigned tref = ct();
    for (int s = 0; s < NSEG - 1; ++s) {
        const int so = (s & 3) * SLOT;
        if (MODE == 1) wait_slot(slot); else if (s > 0) wait_until(tref + tau);
        XSTAMP(0);
        u32x4 m0, m1;
        VLD16(m0, vm0, so); VLD16(m1, vm1, so);
        VWAIT();
        f32x4 te0, to0, te1, to1; unsigned chk;
#define X_HEAD() asm volatile( \
            "v_mfma_f32_16x16x32_bf16 %0, %5, %13, 0\n\tv_mfma_f32_16x16x32_bf16 %1, %6, %13, 0\n\t" \
            "v_max3_u32 %4, %15, %16, %17\n\t" \
            "v_mfma_f32_16x16x32_bf16 %2, %9, %13, 0\n\t" \
            "v_max3_u32 %4, %4, %18, %19\n\t" \
            "v_mfma_f32_16x16x32_bf16 %3, %10, %13, 0\n\t" \
            "v_max3_u32 %4, %4, %20, %21\n\t" \
            "v_mfma_f32_16x16x32_bf16 %0, %7, %14, %0\n\t" \
            "v_max_u32 %4, %4, %22\n\t" \
            "v_mfma_f32_16x16x32_bf16 %1, %8, %14, %1\n\tv_mfma_f32_16x16x32_bf16 %2, %11, %14, %2\n\tv_mfma_f32_16x16x32_bf16 %3, %12, %14, %3" \
            : "=&v"(te0), "=&v"(to0), "=&v"(te1), "=&v"(to1), "=&v"(chk) \
            : "v"(Z00), "v"(Z01), "v"(Z02), "v"(Z03), "v"(Z10), "v"(Z11), "v"(Z12), "v"(Z13), "v"(m0), "v"(m1), \
              "v"(m0.x), "v"(m0.y), "v"(m0.z), "v"(m0.w), "v"(m1.x), "v"(m1.y), "v"(m1.z), "v"(m1.w))
        X_HEAD();
        int att = 0;
        if (__any(chk == SENT)) {
#pragma unroll 1
            for (att = 1;; ++att) {
                if (MODE == 1) { slot = next_slot(slot, p.tauX); wait_slot(slot); }
                asm volatile("buffer_load_dwordx4 %0, %2, %4, %5 offen sc1\n\tbuffer_load_dwordx4 %1, %3, %4, %5 offen sc1\n\ts_waitcnt vmcnt(0)" : "=&v"(m0), "=&v"(m1) : "v"(vm0), "v"(vm1), "s"(rs), "s"(so + MCOPY) : "memory");
                if (!bad2(m0, m1)) break;
                if (att > SPIN_CAP) { note_timeout(rs, 0x500 + s); return; }
            }
            X_HEAD();
            asm volatile("s_nop 1");
        }
        slot += p.tauS + p.tauM + 2 * max(0, RAMPN - s) * RAMP;
        XSTAMP(1);
        f32x4 v;
        {
            float a0, a1, a2, a3, b0, b1, b2, b3;
            asm volatile(
                "s_nop 5\n\t"
                "v_cndmask_b32 %4, %[e10], %[o00], %[pm]\n\tv_cndmask_b32 %5, %[e11], %[o01], %[pm]\n\tv_cndmask_b32 %6, %[e12], %[o02], %[pm]\n\tv_cndmask_b32 %7, %[e13], %[o03], %[pm]\n\t"
                "v_cndmask_b32 %0, %[e00], %[o10], %[pm]\n\tv_cndmask_b32 %1, %[e01], %[o11], %[pm]\n\tv_cndmask_b32 %2, %[e02], %[o12], %[pm]\n\tv_cndmask_b32 %3, %[e03], %[o13], %[pm]\n\t"
                "v_add_f32_dpp %0, %4, %0 row_ror:8 row_mask:0xf bank_mask:0xf\n\tv_add_f32_dpp %1, %5, %1 row_ror:8 row_mask:0xf bank_mask:0xf\n\t"
                "v_add_f32_dpp %2, %6, %2 row_ror:8 row_mask:0xf bank_mask:0xf\n\tv_add_f32_dpp %3, %7, %3 row_ror:8 row_mask:0xf bank_mask:0xf"
                : "=&v"(a0), "=&v"(a1), "=&v"(a2), "=&v"(a3), "=&v"(b0), "=&v"(b1), "=&v"(b2), "=&v"(b3)
                : [pm] "s"(parm),
                  [e00] "v"(te0[0]), [e01] "v"(te0[1]), [e02] "v"(te0[2]), [e03] "v"(te0[3]), [o00] "v"(to0[0]), [o01] "v"(to0[1]), [o02] "v"(to0[2]), [o03] "v"(to0[3]),
                  [e10] "v"(te1[0]), [e11] "v"(te1[1]), [e12] "v"(te1[2]), [e13] "v"(te1[3]), [o10] "v"(to1[0]), [o11] "v"(to1[1]), [o12] "v"(to1[2]), [o13] "v"(to1[3]));
            v = f32x4{a0, a1, a2, a3};
        }
        *(LAS f32x4*)lds_w = v;
        if (MODE == 1) *(LAS unsigned*)(L + L_T + w * 4) = slot;
        lds_barrier();
        {
            const float sv = ((xp[0] + xp[256]) + (xp[512] + xp[768])) * SC2;
            float mx, t, e, sum, pn, lg;
            asm volatile(
                "s_nop 1\n\t"
                "v_max_f32_dpp %0, %4, %4 row_ror:1 row_mask:0xf bank_mask:0xf\n\ts_nop 1\n\t"
                "v_max_f32_dpp %0, %0, %0 row_ror:2 row_mask:0xf bank_mask:0xf\n\ts_nop 1\n\t"
                "v_max_f32_dpp %0, %0, %0 row_ror:4 row_mask:0xf bank_mask:0xf\n\ts_nop 1\n\t"
                "v_max_f32_dpp %0, %0, %0 row_ror:8 row_mask:0xf bank_mask:0xf\n\t"
                "v_mov_b32 %1, %0\n\ts_nop 1\n\t"
                "v_permlane16_swap_b32 %0, %1\n\t"
                "v_max_f32 %0, %0, %1\n\t"
                "v_sub_f32 %2, %4, %0\n\t"
                "v_exp_f32 %2, %2\n\ts_nop 1\n\t"
                "v_add_f32_dpp %3, %2, %2 row_ror:1 row_mask:0xf bank_mask:0xf\n\ts_nop 1\n\t"
                "v_add_f32_dpp %3, %3, %3 row_ror:2 row_mask:0xf bank_mask:0xf\n\ts_nop 1\n\t"
                "v_add_f32_dpp %3, %3, %3 row_ror:4 row_mask:0xf bank_mask:0xf\n\ts_nop 1\n\t"
                "v_add_f32_dpp %3, %3, %3 row_ror:8 row_mask:0xf bank_mask:0xf\n\t"
                "v_mov_b32 %1, %3\n\ts_nop 1\n\t"
                "v_permlane16_swap_b32 %3, %1\n\t"
                "v_add_f32 %3, %3, %1"
                : "=&v"(mx), "=&v"(t), "=&v"(e), "=&v"(sum) : "v"(sv));
            pn = e * __builtin_amdgcn_rcpf(sum);
            const unsigned pv = min(pk2(dpp<QP0022>(pn), dpp<QP1133>(pn)), SENT - 1);
            VST4(pv, vppub, so);
            lg = __builtin_amdgcn_logf(sum);
            const float lse = (mx + lg) * (1.0f / SC2);
            const float hi = __uint_as_float(pk2(lse, 0.f) << 16), r1 = lse - hi, mid = __uint_as_float(pk2(r1, 0.f) << 16), lo = r1 - mid;
            const unsigned d0 = pk2(hi, mid), d1 = pk2(lo, 0.f);
            const unsigned lv = min((key & 3) == 0 ? d0 : ((key & 3) == 1 ? d1 : 0u), SENT - 1);
            VST4(lv, vlpub, so);
        }
        XSTAMP(2);
        if (MODE == 1) slot = slot_max(L + L_T);
        else { tref = ct(); if (s > 0) tau += att == 0 ? -XDN : (att == 1 ? XUP : 4 * XUP); }
        if (s >= 8) natt += att + 1;
        lds_barrier();
        __builtin_amdgcn_s_sleep(2);
        { const int sp = ((s - 1) & 3) * SLOT; VST4(SENTV, vppub, sp); VST4(SENTV, vlpub, sp); }
        if (__any(flg == 0)) { WAIT_FLAG(s + 1); }
        LOAD_Z((s + 1) * (16 * 28 * 1024));
        VLD4(flg, vflag, min(s + 2, NSEG - 1) * 1024);
    }
    DRAIN();
}

constexpr int XB = 32768;
DI void x_dma(const Params& p, int s, LAS unsigned char* buf, int w, int l) {
#pragma unroll
    for (int i = 0; i < 8; ++i) {
        const int r = 8 * w + i;
        int ch = l ^ (r & 15);
        ch = ch < 56 ? ch : ch - 8;
        __builtin_amdgcn_global_load_lds((const GAS unsigned*)(p.x + ((size_t)s * 32 + r) * D + ch * 8), (LAS unsigned*)(buf + r * 1024), 16, 0, 0);
    }
}
DI u32x4 swz_lo(u32x2& a, u32x2& b) {
    const auto x = __builtin_amdgcn_permlane32_swap(a.x, b.x, false, false);
    const auto y = __builtin_amdgcn_permlane32_swap(a.y, b.y, false, false);
    return u32x4{x[0], y[0], x[1], y[1]};
}
template <int NKS> DI void load_tr(u32x4 (&W)[NKS], const u16* src, int ld, LAS unsigned char* stg, int l) {
    constexpr int HALF = NKS / 2;
#pragma unroll
    for (int hf = 0; hf < 2; ++hf) {
        asm volatile("s_waitcnt lgkmcnt(0)" ::: "memory");
        for (int i = 0; i < HALF; ++i) {
            const int r = 16 * i + (l >> 2);
            *(LAS u32x4*)(stg + r * 64 + (l & 3) * 16) = gld16(src + (size_t)(hf * HALF * 16 + r) * ld + (l & 3) * 8);
        }
        asm volatile("s_waitcnt lgkmcnt(0)" ::: "memory");
#pragma unroll
        for (int k = 0; k < HALF; ++k) {
            const LAS unsigned char* a = stg + (16 * k + 8 * (l >> 5) + ((l & 15) >> 2)) * 64 + ((l >> 4) & 1) * 32 + (l & 3) * 8;
            const u32x2 x = __builtin_bit_cast(u32x2, __builtin_amdgcn_ds_read_tr16_b64_v4i16((LAS s16x4*)a));
            const u32x2 y = __builtin_bit_cast(u32x2, __builtin_amdgcn_ds_read_tr16_b64_v4i16((LAS s16x4*)(a + 4 * 64)));
            W[hf * HALF + k] = u32x4{x.x, x.y, y.x, y.y};
        }
    }
}

DI void x_dma1(const Params& p, int s, LAS unsigned char* buf, int r, int ch8) {
    __builtin_amdgcn_global_load_lds((const GAS unsigned*)(p.x + ((size_t)s * 32 + r) * D + ch8), (LAS unsigned*)(buf + r * 1024), 16, 0, 0);
}
template <int KIND>
DI void stream_x(const Params& p, int cu, LAS unsigned char* L) {
    const int tid = threadIdx.x, l = tid & 63, w = __builtin_amdgcn_readfirstlane(tid >> 6), hi = l >> 5, tk = l & 31;
    const rsrc_t rs = __builtin_amdgcn_make_buffer_rsrc(p.ws, 0, (int)WS_END, 0x00020000);
    const int wid = cu * 4 + w;
    u32x4 W[2][28];
    int h = 0, b = 0;
    x_dma(p, 0, L, w, l);
    if (KIND < 2) {
        const int T = wid * 2; h = T / 14; b = T % 14;
        const unsigned char* src = p.ws + (KIND == 0 ? OFF_A : OFF_BT);
#pragma unroll
        for (int ks = 0; ks < 28; ++ks)
#pragma unroll
            for (int t = 0; t < 2; ++t) W[t][ks] = gld16(src + piece(h, b + t, 2 * ks + hi, tk));
        SCHED();
    } else {
        LAS unsigned char* stg = L + 2 * XB + w * (14 * 16 * 64);
        load_tr<28>(W[0], p.Wg + 32 * wid, FF, stg, l);
        load_tr<28>(W[1], p.Wu + 32 * wid, FF, stg, l);
    }
    const int vflag = KIND == 2 ? (int)OFF_FLAG_H + wid * 4 : (int)OFF_FLAG_ZU + ((KIND == 1 ? 112 : 0) + wid) * 4;
    const int fstride = KIND == 2 ? 256 : 1024;
    constexpr int NST = KIND == 2 ? 2 : 4;
    const LAS unsigned char* xrow = L + tk * 1024;
    int xo[8], ch8[8];
#pragma unroll
    for (int k = 0; k < 8; ++k) { xo[k] = ((2 * k + hi) ^ (tk & 15)) * 16; int ch = l ^ ((8 * w + k) & 15); ch8[k] = (ch < 56 ? ch : ch - 8) * 8; }
    const int vst = KIND == 0 ? (int)OFF_Z + ((h * 2 + (tk >> 4)) * 14 + b) * 1024 + (hi * 16 + (tk & 15)) * 16
                  : KIND == 1 ? (int)OFF_U + ((h * 5 + hi) * D + 32 * b + tk) * 16 : (int)OFF_H + ((4 * wid + hi) * 32 + tk) * 16;
    constexpr int sstr = KIND == 0 ? 16 * 28 * 1024 : KIND == 1 ? 16 * 5 * D * 16 : 224 * 512;
    auto epi = [&](const f32x16 (&a)[2], int s, int i) {
        if (KIND == 0) {
            const int t = i >> 1, P = i & 1;
            const u32x4 v = {pk2(a[t][4 * P + 0], a[t][4 * P + 1]), pk2(a[t][4 * P + 2], a[t][4 * P + 3]), pk2(a[t][8 + 4 * P + 0], a[t][8 + 4 * P + 1]), pk2(a[t][8 + 4 * P + 2], a[t][8 + 4 * P + 3])};
            bst16(v, rs, vst + t * 1024 + P * 512, s * sstr);
        } else if (KIND == 1) {
            const int t = i >> 1, P = i & 1;
            u32x2 x = {pk2(a[t][8 * P + 0], a[t][8 * P + 1]), pk2(a[t][8 * P + 2], a[t][8 * P + 3])};
            u32x2 y = {pk2(a[t][8 * P + 4], a[t][8 * P + 5]), pk2(a[t][8 * P + 6], a[t][8 * P + 7])};
            const u32x4 v = swz_lo(x, y);
            bst16(v, rs, vst + t * 512 + P * (2 * D * 16), s * sstr);
        } else {
            float hv[8];
#pragma unroll
            for (int k = 0; k < 8; ++k) { const float gg = a[0][8 * i + k]; hv[k] = gg * __builtin_amdgcn_rcpf(1.f + __builtin_amdgcn_exp2f(-1.4426950408889634f * gg)) * a[1][8 * i + k]; }
            u32x2 x = {pk2(hv[0], hv[1]), pk2(hv[2], hv[3])}, y = {pk2(hv[4], hv[5]), pk2(hv[6], hv[7])};
            const u32x4 v = swz_lo(x, y);
            bst16(v, rs, vst + i * 1024, s * sstr);
        }
    };
    auto tile = [&](int s, f32x16 (&acc)[2], const f32x16 (&prev)[2], bool first = false) {
        if (first && KIND < 2) asm volatile("s_waitcnt vmcnt(56)" ::: "memory");
        else if (s < 4) { DRAIN(); if (s >= 2 && l == 0) bst4(1u, rs, vflag, (s - 2) * fstride); }
        else { asm volatile("s_waitcnt vmcnt(%0)" :: "n"(NST) : "memory"); if (l == 0) { bst4(1u, rs, vflag, (s - 3) * fstride); if (s == 4) bst4(1u, rs, vflag, 2 * fstride); } }
        lds_barrier();
        const LAS unsigned char* xb = xrow + (s & 1) * XB;
        LAS unsigned char* nb = L + ((s + 1) & 1) * XB;
        const int sn = min(s + 1, NSEG - 1);
        u32x4 xf[4];
#pragma unroll
        for (int k = 0; k < 3; ++k) xf[k] = *(const LAS u32x4*)(xb + xo[k]);
#pragma unroll
        for (int ks = 0; ks < 28; ++ks) {
            if (ks + 3 < 28) xf[(ks + 3) & 3] = *(const LAS u32x4*)(xb + xo[(ks + 3) & 7] + ((ks + 3) >> 3) * 256);
            SCHED();
            if (ks == 0) {
                if (KIND == 1) { mma32_wb<true>(acc[0], xf[0], W[0][0]); mma32_wb<true>(acc[1], xf[0], W[1][0]); }
                else { mma32_wa<true>(acc[0], W[0][0], xf[0]); mma32_wa<true>(acc[1], W[1][0], xf[0]); }
            } else {
                if (KIND == 1) { mma32_wb<false>(acc[0], xf[ks & 3], W[0][ks]); mma32_wb<false>(acc[1], xf[ks & 3], W[1][ks]); }
                else { mma32_wa<false>(acc[0], W[0][ks], xf[ks & 3]); mma32_wa<false>(acc[1], W[1][ks], xf[ks & 3]); }
            }
            SCHED();
            if (ks >= 1 && ks < 9) x_dma1(p, sn, nb, 8 * w + ks - 1, ch8[ks - 1]);
            if (ks >= 10 && (ks - 10) % (KIND == 2 ? 8 : 4) == 0 && (ks - 10) / (KIND == 2 ? 8 : 4) < NST && s > 1) epi(prev, s - 1, (ks - 10) / (KIND == 2 ? 8 : 4));
            SCHED();
        }
    };
    f32x16 accA[2], accB[2];
    tile(0, accB, accA, true);
    mma32_done(accB[0], accB[1]);
#pragma unroll
    for (int i = 0; i < NST; ++i) epi(accB, 0, i);
    DRAIN();
    if (l == 0) bst4(1u, rs, vflag, 0);
    tile(1, accB, accA);
#pragma unroll 1
    for (int s = 2; s < NSEG; s += 2) { tile(s, accA, accB); tile(s + 1, accB, accA); }
    mma32_done(accB[0], accB[1]);
#pragma unroll
    for (int i = 0; i < NST; ++i) epi(accB, NSEG - 1, i);
    DRAIN();
    if (l == 0) { bst4(1u, rs, vflag, (NSEG - 3) * fstride); bst4(1u, rs, vflag, (NSEG - 2) * fstride); bst4(1u, rs, vflag, (NSEG - 1) * fstride); }
}

DI void ffn_down(const Params& p, int cu14, LAS unsigned char* L) {
    const int cu = cu14 % 7, grp = cu14 / 7;
    const int tid = threadIdx.x, l = tid & 63, w = __builtin_amdgcn_readfirstlane(tid >> 6), hi = l >> 5, tk = l & 31;
    const rsrc_t rs = __builtin_amdgcn_make_buffer_rsrc(p.ws, 0, (int)WS_END, 0x00020000);
    u32x4 W[2][28];
    {
        LAS unsigned char* stg = L + w * (14 * 16 * 64);
        load_tr<28>(W[0], p.Wd + (size_t)(448 * w) * D + 64 * cu, D, stg, l);
        load_tr<28>(W[1], p.Wd + (size_t)(448 * w) * D + 64 * cu + 32, D, stg, l);
    }
    lds_barrier();
    const int vh = (int)OFF_H + (56 * w) * 512 + l * 16;
    const int vflag = (int)OFF_FLAG_H + (14 * w + min(l, 13)) * 4;
    LAS float* red = (LAS float*)L;
    float* fo = (float*)(p.ws + OFF_FO);
    auto wait_h = [&](int s) -> bool {
        for (int att = 0;; ++att) {
            asm volatile("" ::: "memory");
            const unsigned f = bld4(rs, vflag, s * 256);
            if (!__any(f == 0)) return true;
            if (att > SPIN_CAP) { note_timeout(rs, 0x600 + s); return false; }
            __builtin_amdgcn_s_sleep(8);
        }
    };
    if (!wait_h(grp)) return;
    u32x4 hf[28];
#pragma unroll
    for (int ks = 0; ks < 28; ++ks) hf[ks] = bld16(rs, vh + ks * 1024, grp * (224 * 512));
    for (int s = grp; s < NSEG; s += 2) {
        const int sn = min(s + 2, NSEG - 2 + grp);
        if (!wait_h(sn)) return;
        f32x16 acc[2];
#pragma unroll
        for (int ks = 0; ks < 28; ++ks) {
            if (ks == 0) { mma32_wb<true>(acc[0], hf[0], W[0][0]); mma32_wb<true>(acc[1], hf[0], W[1][0]); }
            else { mma32_wb<false>(acc[0], hf[ks], W[0][ks]); mma32_wb<false>(acc[1], hf[ks], W[1][ks]); }
            SCHED();
            hf[ks] = bld16(rs, vh + ks * 1024, sn * (224 * 512));
            SCHED();
        }
        mma32_done(acc[0], acc[1]);
        lds_barrier();
#pragma unroll
        for (int t = 0; t < 2; ++t)
#pragma unroll
            for (int i = 0; i < 16; ++i) red[((w * 2 + t) * 16 + i) * 64 + l] = acc[t][i];
        lds_barrier();
#pragma unroll
        for (int t = 0; t < 2; ++t)
#pragma unroll
            for (int r = 0; r < 4; ++r) {
                const int i = 4 * w + r;
                const float v = (red[((0 * 2 + t) * 16 + i) * 64 + l] + red[((1 * 2 + t) * 16 + i) * 64 + l]) + (red[((2 * 2 + t) * 16 + i) * 64 + l] + red[((3 * 2 + t) * 16 + i) * 64 + l]);
                bst4(__float_as_uint(v), rs, (int)OFF_FO + ((8 * w + 4 * hi + r) * D + 64 * cu + 32 * t + tk) * 4, s * (32 * D * 4));
            }
        DRAIN();
        if (l == 0) bst4(1u, rs, (int)OFF_FLAG_F + (cu * 4 + w) * 4, s * 128);
    }
}

DI bool wait_flags(rsrc_t rs, int voff, int soff, unsigned code) {
#pragma unroll 1
    for (int att = 0;; ++att) {
        asm volatile("" ::: "memory");
        const unsigned f = bld4(rs, voff, soff);
        if (!__any(f == 0)) return true;
        if (att > SPIN_CAP) { note_timeout(rs, code); return false; }
        __builtin_amdgcn_s_sleep(POLL_SLEEP);
    }
}
DI bool wait_data(rsrc_t rs, int voff, int soff, unsigned code) {
#pragma unroll 1
    for (int att = 0;; ++att) {
        asm volatile("" ::: "memory");
        const unsigned f = bld4(rs, voff, soff);
        if (!__any(f == SENT)) return true;
        if (att > SPIN_CAP) { note_timeout(rs, code); return false; }
        __builtin_amdgcn_s_sleep(POLL_SLEEP);
    }
}
DI void rows_role(const Params& p, int id, LAS unsigned char* L) {
    const int tid = threadIdx.x, l = tid & 63, w = __builtin_amdgcn_readfirstlane(tid >> 6), r16 = l & 15, q = l >> 4;
    const int grp = id >> 2, qi = id & 3;
    const rsrc_t rs = __builtin_amdgcn_make_buffer_rsrc(p.ws, 0, (int)WS_END, 0x00020000);
    LAS f32x4* red = (LAS f32x4*)(L + XB);
    LAS float* gate = (LAS float*)(L + XB + 4 * 14 * 1024);
    const int h = 4 * qi + w;
    const int k0 = 8 * (r16 >> 2) + (r16 & 3), k1 = k0 + 4;
    const int vz0 = (int)OFF_Z + h * 28 * 1024 + (k0 >> 4) * 14 * 1024 + (q * 16 + (k0 & 15)) * 16, vz1 = (int)OFF_Z + h * 28 * 1024 + (k1 >> 4) * 14 * 1024 + (q * 16 + (k1 & 15)) * 16;
    const int vzm = (int)OFF_ZM + h * 14 * 512 + q * 128 + (r16 & 7) * 16;
#pragma unroll 1
    for (int s = grp; s < NSEG; s += 11) {
        lds_barrier();
        x_dma(p, s, L, w, l);
        if (!wait_flags(rs, (int)OFF_FLAG_ZU + (7 * h + min(l, 6)) * 4, s * 1024, 0x700 + s)) return;
        u32x4 z0[14], z1[14], zm[14];
#pragma unroll
        for (int ks = 0; ks < 14; ++ks) { z0[ks] = bld16(rs, vz0 + ks * 1024, s * (16 * 28 * 1024)); z1[ks] = bld16(rs, vz1 + ks * 1024, s * (16 * 28 * 1024)); }
        if (!wait_data(rs, (int)OFF_M + ((7 * h + min(l, 6)) % 56) * 128 + ((7 * h + min(l, 6)) / 56) * 64, (s + 2) * 7168, 0x800 + s)) return;
#pragma unroll
        for (int ks = 0; ks < 14; ++ks) zm[ks] = bld16(rs, vzm + ks * 512, s * (16 * 14 * 512));
        DRAIN();
        lds_barrier();
        f32x4 sc[3][2];
#pragma unroll
        for (int i = 0; i < 3; ++i) { sc[i][0] = f32x4{0, 0, 0, 0}; sc[i][1] = sc[i][0]; }
#pragma unroll
        for (int ks = 0; ks < 14; ++ks)
#pragma unroll
            for (int it = 0; it < 2; ++it) {
                const u32x4 xf = *(const LAS u32x4*)(L + (16 * it + r16) * 1024 + (((4 * ks + q) & 48) | (((4 * ks + q) & 15) ^ r16)) * 16);
                sc[0][it] = mfma16(z0[ks], xf, sc[0][it]); sc[1][it] = mfma16(z1[ks], xf, sc[1][it]); sc[2][it] = mfma16(zm[ks], xf, sc[2][it]);
            }
#pragma unroll
        for (int it = 0; it < 2; ++it) {
            const int i = 16 * it + r16;
            float v[12];
#pragma unroll
            for (int r = 0; r < 4; ++r) {
                v[r] = (8 * q + r <= i) ? sc[0][it][r] * SC2 : -3.0e38f;
                v[4 + r] = (8 * q + 4 + r <= i) ? sc[1][it][r] * SC2 : -3.0e38f;
                v[8 + r] = q < 2 ? sc[2][it][r] * SC2 : -3.0e38f;
            }
            float mx = v[0];
#pragma unroll
            for (int k = 1; k < 12; ++k) mx = fmaxf(mx, v[k]);
            mx = fmaxf(mx, __shfl_xor(mx, 16)); mx = fmaxf(mx, __shfl_xor(mx, 32));
            float sum = 0;
#pragma unroll
            for (int k = 0; k < 12; ++k) { v[k] = __builtin_amdgcn_exp2f(v[k] - mx); sum += v[k]; }
            sum += __shfl_xor(sum, 16); sum += __shfl_xor(sum, 32);
            const float rl = 1.0f / sum;
            const u32x4 px = {pk2(v[0] * rl, v[1] * rl), pk2(v[2] * rl, v[3] * rl), pk2(v[4] * rl, v[5] * rl), pk2(v[6] * rl, v[7] * rl)};
            bst16(px, rs, (int)OFF_PX + (h * 2 + it) * 1024 + l * 16, s * (16 * 2 * 1024));
            const u32x2 pm = {pk2(v[8] * rl, v[9] * rl), pk2(v[10] * rl, v[11] * rl)};
            bst8(pm, rs, (int)OFF_PM + (h * 2 + it) * 512 + l * 8, s * (16 * 2 * 512));
        }
        {
            const int row = tid >> 3, part = tid & 7;
            float g = 0;
#pragma unroll
            for (int k = 0; k < 7; ++k) {
                const int ch = part * 7 + k;
                const u32x4 a = *(const LAS u32x4*)(L + row * 1024 + ((ch & 48) | ((ch & 15) ^ (row & 15))) * 16), b = gld16(p.gw + ch * 8);
#pragma unroll
                for (int e = 0; e < 4; ++e) g += __uint_as_float(a[e] << 16) * __uint_as_float(b[e] << 16) + __uint_as_float(a[e] & 0xFFFF0000u) * __uint_as_float(b[e] & 0xFFFF0000u);
            }
            g += __shfl_xor(g, 1); g += __shfl_xor(g, 2); g += __shfl_xor(g, 4);
            if (part == 0) gate[row] = 1.0f / (1.0f + __expf(-g));
        }
        DRAIN();
        if (l == 0) bst4(1u, rs, (int)OFF_FLAG_P + (qi * 4 + w) * 4, s * 64);
        const int b0 = (112 * qi) >> 5, b1 = (112 * qi + 111) >> 5;
        if (!wait_flags(rs, (int)OFF_FLAG_ZU + (112 + ((4 * w + ((l >> 3) & 3)) * 14 + min(b0 + (l & 7), b1)) / 2) * 4, s * 1024, 0x900 + s)) return;
        u32x4 uf[4][7];
#pragma unroll
        for (int hh = 0; hh < 4; ++hh)
#pragma unroll
            for (int t = 0; t < 7; ++t) uf[hh][t] = bld16(rs, (int)OFF_U + (((4 * w + hh) * 5 + q) * D + 112 * qi + 16 * t + r16) * 16, s * (16 * 5 * D * 16));
        if (!wait_data(rs, (int)OFF_M + (14 * qi + min(l, 13)) * 128, (s + 2) * 7168, 0xA00 + s)) return;
        u32x2 yf[4][7];
#pragma unroll
        for (int hh = 0; hh < 4; ++hh)
#pragma unroll
            for (int t = 0; t < 7; ++t) yf[hh][t] = bld8(rs, q < 2 ? (int)OFF_U + (((4 * w + hh) * 5 + 4) * D + 112 * qi + 16 * t + r16) * 16 + q * 8 : OOB, s * (16 * 5 * D * 16));
        if (!wait_flags(rs, (int)OFF_FLAG_P + (l & 15) * 4, s * 64, 0xB00 + s)) return;
        f32x4 acc[7][2];
#pragma unroll
        for (int t = 0; t < 7; ++t) { acc[t][0] = f32x4{0, 0, 0, 0}; acc[t][1] = acc[t][0]; }
#pragma unroll
        for (int hh = 0; hh < 4; ++hh) {
            u32x4 px[2]; u32x2 pm[2];
#pragma unroll
            for (int it = 0; it < 2; ++it) {
                px[it] = bld16(rs, (int)OFF_PX + ((4 * w + hh) * 2 + it) * 1024 + l * 16, s * (16 * 2 * 1024));
                pm[it] = bld8(rs, (int)OFF_PM + ((4 * w + hh) * 2 + it) * 512 + l * 8, s * (16 * 2 * 512));
            }
#pragma unroll
            for (int t = 0; t < 7; ++t)
#pragma unroll
                for (int it = 0; it < 2; ++it) { acc[t][it] = mfma16(uf[hh][t], px[it], acc[t][it]); acc[t][it] = mfma16k16(yf[hh][t], pm[it], acc[t][it]); }
        }
#pragma unroll
        for (int t = 0; t < 7; ++t) { red[(w * 14 + 2 * t) * 64 + l] = acc[t][0]; red[(w * 14 + 2 * t + 1) * 64 + l] = acc[t][1]; }
        if (!wait_flags(rs, (int)OFF_FLAG_F + min(l, 27) * 4, s * 128, 0xC00 + s)) return;
        lds_barrier();
        for (int u = w; u < 14; u += 4) {
            const f32x4 a = (red[(0 * 14 + u) * 64 + l] + red[(1 * 14 + u) * 64 + l]) + (red[(2 * 14 + u) * 64 + l] + red[(3 * 14 + u) * 64 + l]);
            const int i = 16 * (u & 1) + r16, f = 112 * qi + 16 * (u >> 1) + 4 * q;
            const float g = gate[i];
            const u32x4 bb = bld16(rs, (int)OFF_FO + (i * D + f) * 4, s * (32 * D * 4));
            const u32x2 o = {pk2(g * a[0] + (1.f - g) * __uint_as_float(bb[0]), g * a[1] + (1.f - g) * __uint_as_float(bb[1])), pk2(g * a[2] + (1.f - g) * __uint_as_float(bb[2]), g * a[3] + (1.f - g) * __uint_as_float(bb[3]))};
            *(GAS u32x2*)(p.out + ((size_t)s * 32 + i) * D + f) = o;
        }
    }
}

__global__ void __launch_bounds__(256, 1) k_main(Params p) {
    extern __shared__ __attribute__((aligned(16))) unsigned char smem[];
    LAS unsigned char* L = (LAS unsigned char*)smem;
    const int b = blockIdx.x, xl = b & 7, idx = (b >> 3) * 4 + (xl & 3);
    if (xl < 4) {
        if (idx < 112) chain_merged(p, idx, L); else chain_mx(p, idx - 112, L);
    } else if (!(p.pad & 1) || idx >= 84) {
        if (idx < 28) stream_x<0>(p, idx, L);
        else if (idx < 56) stream_x<1>(p, idx - 28, L);
        else if (idx < 70) { for (int i = 0; i < FFN_DELAY; ++i) __builtin_amdgcn_s_sleep(127); stream_x<2>(p, idx - 56, L); }
        else if (idx < 84) { for (int i = 0; i < FFN_DELAY; ++i) __builtin_amdgcn_s_sleep(127); ffn_down(p, idx - 70, L); }
        else { if (idx == 127) probe_parity(p); if (!(p.pad & 3)) rows_role(p, idx - 84, L); }
    }
}

#define TAU_M 900
#define TAU_S 800
#define TAU_X 2600
int g_tauM = TAU_M, g_tauS = TAU_S, g_tauX = TAU_X, g_dbg = 0;

extern "C" void kernel_launch(void* const* d_in, const int* in_sizes, int n_in, void* d_out, int out_size, void* d_ws, size_t ws_size, hipStream_t stream) {
    Params p;
    p.x = (const u16*)d_in[0]; p.mem0 = (const u16*)d_in[1]; p.Wq = (const u16*)d_in[2]; p.Wk = (const u16*)d_in[3]; p.Wv = (const u16*)d_in[4]; p.Wo = (const u16*)d_in[5];
    p.Wg = (const u16*)d_in[8]; p.Wu = (const u16*)d_in[9]; p.Wd = (const u16*)d_in[10]; p.gw = (const u16*)d_in[11];
    p.ws = (unsigned char*)(((uintptr_t)d_ws + 2 * MB - 1) & ~(uintptr_t)(2 * MB - 1));
    p.out = (u16*)d_out;
    p.tauM = g_tauM; p.tauS = g_tauS; p.tauX = g_tauX; p.pad = g_dbg;
    constexpr int LDS_MAIN = 2 * XB + 4 * 14 * 16 * 64;
    hipFuncSetAttribute((const void*)k_main, hipFuncAttributeMaxDynamicSharedMemorySize, LDS_MAIN);
    hipFuncSetAttribute((const void*)k_prep, hipFuncAttributeMaxDynamicSharedMemorySize, 65536);
    k_prep<<<NPREP, 256, 65536, stream>>>(p);
    k_main<<<256, 256, LDS_MAIN, stream>>>(p);

}
```

```cpp
#include <hip/hip_runtime.h>
#include <stdint.h>

#define DI __device__ __forceinline__
#define LAS __attribute__((address_space(3)))
#define GAS __attribute__((address_space(1)))
typedef unsigned short u16;
typedef __bf16 bf16x8 __attribute__((ext_vector_type(8)));
typedef short s16x4 __attribute__((ext_vector_type(4)));
typedef float f32x4 __attribute__((ext_vector_type(4)));
typedef float f32x16 __attribute__((ext_vector_type(16)));
typedef unsigned u32x4 __attribute__((ext_vector_type(4)));
typedef unsigned u32x2 __attribute__((ext_vector_type(2)));
typedef __amdgpu_buffer_rsrc_t rsrc_t;

constexpr int D = 448, NH = 16, NSEG = 64, FF = 1792;
constexpr size_t MB = 1u << 20;
constexpr size_t OFF_RING = 0;
constexpr size_t OFF_M = 512 * 1024;
constexpr size_t OFF_PROBE = 1664 * 1024;
constexpr size_t OFF_CTRL = 2 * MB;
constexpr size_t OFF_FLAG_ZU = OFF_CTRL + 4096;
constexpr size_t OFF_FLAG_H = OFF_FLAG_ZU + 64 * 256 * 4;
constexpr size_t OFF_FLAG_F = OFF_FLAG_H + 64 * 64 * 4;
constexpr size_t OFF_FLAG_P = OFF_FLAG_F + 64 * 32 * 4;
constexpr size_t CTRL_END = OFF_FLAG_P + 64 * 16 * 4;
constexpr size_t OFF_STAT = 2 * MB + 512 * 1024;
constexpr size_t SZ_MAT = (size_t)NH * D * D * 2;
constexpr size_t OFF_A = 3 * MB;
constexpr size_t OFF_AT = OFF_A + 7 * MB;
constexpr size_t OFF_BT = OFF_AT + 7 * MB;
constexpr size_t OFF_Z = OFF_BT + 7 * MB;
constexpr size_t OFF_U = OFF_Z + 28 * MB;
constexpr size_t OFF_ZM = OFF_U + 35 * MB;
constexpr size_t OFF_H = OFF_ZM + 7 * MB;
constexpr size_t OFF_FO = OFF_H + 7 * MB;
constexpr size_t OFF_PX = OFF_FO + 4 * MB;
constexpr size_t OFF_PM = OFF_PX + 2 * MB;
constexpr size_t WS_END = OFF_PM + 1 * MB;
static_assert(WS_END + 2 * MB <= 256 * MB, "workspace");

constexpr int AUX_SC1 = 16;
constexpr unsigned SENT = 0xFFFFFFFFu;
constexpr float SC2 = 0.0625f * 1.4426950408889634f;

struct Params {
    const u16* x; const u16* mem0; const u16* Wq; const u16* Wk; const u16* Wv; const u16* Wo;
    const u16* Wg; const u16* Wu; const u16* Wd; const u16* gw;
    unsigned char* ws; u16* out;
    int tauM, tauS, tauX, tauXm, pad, pad2;
};

DI unsigned pk2(float a, float b) { typedef __bf16 bf2 __attribute__((ext_vector_type(2))); bf2 v = {(__bf16)a, (__bf16)b}; return __builtin_bit_cast(unsigned, v); }
DI f32x4 mfma16(u32x4 a, u32x4 b, f32x4 c) { return __builtin_amdgcn_mfma_f32_16x16x32_bf16(__builtin_bit_cast(bf16x8, a), __builtin_bit_cast(bf16x8, b), c, 0, 0, 0); }
DI f32x4 mfma16k16(u32x2 a, u32x2 b, f32x4 c) { return __builtin_amdgcn_mfma_f32_16x16x16bf16_1k(__builtin_bit_cast(s16x4, a), __builtin_bit_cast(s16x4, b), c, 0, 0, 0); }
DI f32x16 mfma32(u32x4 a, u32x4 b, f32x16 c) { return __builtin_amdgcn_mfma_f32_32x32x16_bf16(__builtin_bit_cast(bf16x8, a), __builtin_bit_cast(bf16x8, b), c, 0, 0, 0); }
template <bool FIRST> DI void mma32_wa(f32x16& acc, const u32x4& w, const u32x4& x) {
    if (FIRST) asm volatile("v_mfma_f32_32x32x16_bf16 %0, %1, %2, 0" : "=&v"(acc) : "a"(w), "v"(x));
    else asm volatile("v_mfma_f32_32x32x16_bf16 %0, %1, %2, %0" : "+v"(acc) : "a"(w), "v"(x));
}
template <bool FIRST> DI void mma32_wb(f32x16& acc, const u32x4& x, const u32x4& w) {
    if (FIRST) asm volatile("v_mfma_f32_32x32x16_bf16 %0, %1, %2, 0" : "=&v"(acc) : "v"(x), "a"(w));
    else asm volatile("v_mfma_f32_32x32x16_bf16 %0, %1, %2, %0" : "+v"(acc) : "v"(x), "a"(w));
}
DI void mma32_done(f32x16& a, f32x16& b) { asm volatile("s_nop 7\n\ts_nop 3" : "+v"(a), "+v"(b)); }
DI u32x4 gld16(const void* p) { return *(const GAS u32x4*)p; }
DI u32x4 bld16(rsrc_t r, int voff, int soff) { return __builtin_amdgcn_raw_buffer_load_b128(r, voff, soff, AUX_SC1); }
DI u32x2 bld8(rsrc_t r, int voff, int soff) { return __builtin_amdgcn_raw_buffer_load_b64(r, voff, soff, AUX_SC1); }
DI unsigned bld4(rsrc_t r, int voff, int soff) { return __builtin_amdgcn_raw_buffer_load_b32(r, voff, soff, AUX_SC1); }
DI void bst16(u32x4 v, rsrc_t r, int voff, int soff) { __builtin_amdgcn_raw_buffer_store_b128(v, r, voff + soff, 0, AUX_SC1); }
DI void bst8(u32x2 v, rsrc_t r, int voff, int soff) { __builtin_amdgcn_raw_buffer_store_b64(v, r, voff, soff, AUX_SC1); }
DI void bst4(unsigned v, rsrc_t r, int voff, int soff) { __builtin_amdgcn_raw_buffer_store_b32(v, r, voff, soff, AUX_SC1); }
DI unsigned ct() { return (unsigned)__builtin_amdgcn_s_memtime(); }
DI void wait_until(unsigned t) { while ((int)(ct() - t) < 0) {} }
DI unsigned rt16() { return (unsigned)__builtin_amdgcn_s_memrealtime() << 4; }
DI void wait_slot(unsigned slot) { while ((int)(rt16() - slot) < 0) {} }
DI unsigned slot_max(const LAS unsigned char* w) {
    const u32x4 v = *(const LAS u32x4*)w;
    const unsigned a = (int)(v.x - v.y) > 0 ? v.x : v.y, b = (int)(v.z - v.w) > 0 ? v.z : v.w;
    return __builtin_amdgcn_readfirstlane((int)(a - b) > 0 ? a : b);
}
DI unsigned next_slot(unsigned slot, unsigned q) { const unsigned now = rt16(); do slot += q; while ((int)(now - slot) >= 0); return slot; }
template <int CTRL> DI float dpp(float v) { return __int_as_float(__builtin_amdgcn_update_dpp(0, __float_as_int(v), CTRL, 0xf, 0xf, true)); }
constexpr int ROR1 = 0x121, ROR2 = 0x122, ROR4 = 0x124, ROR8 = 0x128, QP0022 = 0xA0, QP1133 = 0xF5;
DI unsigned mx4(u32x4 v) { return max(max(v.x, v.y), max(v.z, v.w)); }
DI void lds_barrier() { asm volatile("s_waitcnt lgkmcnt(0)" ::: "memory"); __builtin_amdgcn_s_barrier(); asm volatile("" ::: "memory"); }
#define SCHED() __builtin_amdgcn_sched_barrier(0)
#define DRAIN() asm volatile("s_waitcnt vmcnt(0)" ::: "memory")
constexpr int SPIN_CAP = 1 << 18;
#define PRETOUCH 1
#define POLL_SLEEP 16
DI void note_timeout(rsrc_t rs, unsigned code) { bst4(code, rs, (int)OFF_CTRL + 64, 0); }

DI int pidx(int i, int r) { return 32 * (i >> 1) + 8 * (r >> 2) + 4 * (i & 1) + (r & 3); }

constexpr int NPREP = 512;
#define PREP_SC1 1
#define WARM 1
DI size_t piece(int h, int blk, int oct, int pos) { return ((((size_t)h * 14 + blk) * 56 + oct) * 32 + pos) * 16; }

__global__ void __launch_bounds__(256, 2) k_prep(Params p) {
    extern __shared__ __attribute__((aligned(16))) unsigned char smem[];
    const int tid = threadIdx.x, l = tid & 63, w = __builtin_amdgcn_readfirstlane(tid >> 6), r16 = l & 15, q = l >> 4;
    const int blk = blockIdx.x;
#define PSTAMP(x)
    {
        if (w == 0) {
            const int i = blk * 64 + l, fb = i >> 10, o = i & 511;
            u32x4 v = {SENT, SENT, SENT, SENT};
            if (fb < 2 && o < 448) { const int b = o >> 3, t = o & 7; v = gld16(p.mem0 + t * D + 8 * b); v.x = min(v.x, SENT - 1); v.y = min(v.y, SENT - 1); v.z = min(v.z, SENT - 1); v.w = min(v.w, SENT - 1); }
            *(GAS u32x4*)(p.ws + OFF_RING + (size_t)i * 16) = v;
        } else if (w & 1) {
            constexpr int NW = (int)((CTRL_END - OFF_CTRL) / 4);
            static_assert(NW <= 2 * NPREP * 64, "flags");
            const int i = (blk * 2 + (w >> 1)) * 64 + l;
            if (i < NW && (!(p.pad & 1) || i >= (int)((OFF_FLAG_F - OFF_CTRL) / 4) || i < 16)) ((GAS unsigned*)(p.ws + OFF_CTRL))[i] = 0;
        } else if (w == 2) {
#pragma unroll
            for (int n = 0; n < 3; ++n) {
                const int i = (blk * 3 + n) * 64 + l, o = i & 511;
                u32x4 v = {SENT, SENT, SENT, SENT};
                if (i < 1024 && o < 448) v = gld16(p.mem0 + (o & 7) * D + 8 * (o >> 3));
                if (i < 67 * 1024) *(GAS u32x4*)(p.ws + OFF_M + (size_t)i * 16) = v;
            }
        }
        {
            u32x4 t[7];
#pragma unroll
            for (int n = 0; n < 7; ++n) {
                const int idx = blk * 256 + tid + n * (NPREP * 256), m = idx / 229376, o = idx % 229376;
                const u16* base = m == 0 ? p.Wq : m == 1 ? p.Wk : m == 2 ? p.Wv : p.Wo;
                t[n] = gld16(base + (size_t)o * 8);
            }
#pragma unroll
            for (int n = 0; n < 7; ++n) asm volatile("" :: "v"(t[n]));
        }
        PSTAMP(t1);
        const int T = blk >> 3;
        const bool tA = T < 32;
        const int h = 2 * (blk & 7) + ((T >> 4) & 1), tr = (T >> 2) & 3, tc = T & 3;
        const int wr = w >> 1, wc = w & 1, rt = 2 * tr + wr, ctl = 2 * tc + wc;
        const bool active = rt < 7 && ctl < 7;
        LAS unsigned char* L = (LAS unsigned char*)smem;
        const char* baseR = (const char*)(tA ? p.Wq : p.Wv); const char* baseC = (const char*)(tA ? p.Wk : p.Wo);
        unsigned offR[4], offC[4];
#pragma unroll
        for (int n = 0; n < 4; ++n) {
            const int rho = 8 * (4 * w + n) + (l >> 3), ch = (l & 7) ^ (rho & 7);
            const int rr = min(128 * tr + 64 * (rho >> 6) + pidx((rho & 63) >> 4, rho & 15), D - 1);
            offR[n] = ((rr * 16 + h) * 256 + ch * 8) * 2;
            if (tA) { const int cc = min(128 * tc + 64 * (rho >> 6) + pidx((rho & 63) >> 4, rho & 15), D - 1); offC[n] = ((cc * 16 + h) * 256 + ch * 8) * 2; }
            else { const int e = 4 * (4 * w + n) + (l >> 4), c16 = (l & 15) ^ ((e & 7) << 1); offC[n] = ((h * 256 + e) * D + min(128 * tc + 8 * c16, D - 8)) * 2; }
        }
        auto dma = [&](int kc) {
            LAS unsigned char* b = L + (kc & 1) * 32768;
            const char* bR = baseR + 128 * kc; const char* bC = baseC + (tA ? 128 * kc : 128 * kc * D);
#pragma unroll
            for (int n = 0; n < 4; ++n) {
                __builtin_amdgcn_global_load_lds((const GAS unsigned*)(bR + offR[n]), (LAS unsigned*)(b + (4 * w + n) * 1024), 16, 0, 0);
                __builtin_amdgcn_global_load_lds((const GAS unsigned*)(bC + offC[n]), (LAS unsigned*)(b + 16384 + (4 * w + n) * 1024), 16, 0, 0);
            }
        };
        f32x4 a1[4][4], a2[4][4];
#pragma unroll
        for (int i = 0; i < 4; ++i)
#pragma unroll
            for (int j = 0; j < 4; ++j) { a1[i][j] = f32x4{0, 0, 0, 0}; a2[i][j] = a1[i][j]; }
        dma(0); dma(1);
#pragma unroll 1
        for (int kc = 0; kc < 4; ++kc) {
            if (kc < 3) asm volatile("s_waitcnt vmcnt(8)" ::: "memory"); else DRAIN();
            lds_barrier();
            const LAS unsigned char* b = L + (kc & 1) * 32768;
            if (active) {
#pragma unroll
                for (int k2 = 0; k2 < 2; ++k2) {
                    u32x4 fa[4], fb[4];
#pragma unroll
                    for (int i = 0; i < 4; ++i) { const int rho = 64 * wr + 16 * i + r16; fa[i] = *(const LAS u32x4*)(b + rho * 128 + ((4 * k2 + q) ^ (rho & 7)) * 16); }
                    if (tA) {
#pragma unroll
                        for (int j = 0; j < 4; ++j) { const int rho = 64 * wc + 16 * j + r16; fb[j] = *(const LAS u32x4*)(b + 16384 + rho * 128 + ((4 * k2 + q) ^ (rho & 7)) * 16); }
#pragma unroll
                        for (int i = 0; i < 4; ++i)
#pragma unroll
                            for (int j = 0; j < 4; ++j) { a1[i][j] = mfma16(fa[i], fb[j], a1[i][j]); a2[j][i] = mfma16(fb[j], fa[i], a2[j][i]); }
                    } else {
#pragma unroll
                        for (int j = 0; j < 4; ++j) {
                            const int e0 = 32 * k2 + 8 * q + (r16 >> 2), c16 = 8 * wc + 2 * j + ((l & 3) >> 1);
                            const s16x4 lo = __builtin_amdgcn_ds_read_tr16_b64_v4i16((LAS s16x4*)(b + 16384 + e0 * 256 + (c16 ^ ((e0 & 7) << 1)) * 16 + (l & 1) * 8));
                            const s16x4 hi = __builtin_amdgcn_ds_read_tr16_b64_v4i16((LAS s16x4*)(b + 16384 + (e0 + 4) * 256 + (c16 ^ (((e0 + 4) & 7) << 1)) * 16 + (l & 1) * 8));
                            const u32x2 x = __builtin_bit_cast(u32x2, lo), y = __builtin_bit_cast(u32x2, hi);
                            fb[j] = u32x4{x.x, x.y, y.x, y.y};
                        }
#pragma unroll
                        for (int i = 0; i < 4; ++i)
#pragma unroll
                            for (int j = 0; j < 4; ++j) a1[i][j] = mfma16(fa[i], fb[j], a1[i][j]);
                    }
                }
            }
            if (kc < 2) { lds_barrier(); dma(kc + 2); }
        }
        PSTAMP(t2);
        const rsrc_t rs = __builtin_amdgcn_make_buffer_rsrc(p.ws, 0, (int)WS_END, 0x00020000);
#define PST(v, off) bst16(v, rs, (int)(off), 0)
        if (active) {
#pragma unroll
            for (int P = 0; P < 2; ++P)
#pragma unroll
                for (int j = 0; j < 4; ++j) {
                    const u32x4 v1 = {pk2(a1[2 * P][j][0], a1[2 * P][j][1]), pk2(a1[2 * P][j][2], a1[2 * P][j][3]), pk2(a1[2 * P + 1][j][0], a1[2 * P + 1][j][1]), pk2(a1[2 * P + 1][j][2], a1[2 * P + 1][j][3])};
                    PST(v1, (tA ? OFF_AT : OFF_BT) + piece(h, 2 * ctl + (j >> 1), 8 * rt + 4 * P + q, 16 * (j & 1) + r16));
                    if (tA) {
                        const u32x4 v2 = {pk2(a2[2 * P][j][0], a2[2 * P][j][1]), pk2(a2[2 * P][j][2], a2[2 * P][j][3]), pk2(a2[2 * P + 1][j][0], a2[2 * P + 1][j][1]), pk2(a2[2 * P + 1][j][2], a2[2 * P + 1][j][3])};
                        PST(v2, OFF_A + piece(h, 2 * rt + (j >> 1), 8 * ctl + 4 * P + q, 16 * (j & 1) + r16));
                    }
                }
        }
    }
}

DI int s_phys(int o) { return (o >> 13) * 16384 + (o & 8191); }
DI int s_piece(int h, int pc, int m) { return s_phys((((m >> 2) * 4 + (h >> 2)) * 3 + (pc >> 2)) * 1024 + (pc & 3) * 256 + (h & 3) * 64 + (m & 3) * 16); }
constexpr int SLOT = 8 * 16384, MCOPY = 16384, SOFF = 2 * 16384, SCOPY = 3 * 16384;
constexpr int L_M = 0;
constexpr int L_ZERO = 7168;
constexpr int L_S = 8192;
constexpr int L_O = 9216;
constexpr int L_X = 10240;
constexpr int L_DUMMY = 14336;
constexpr int L_T = 15360;

DI bool bad2(u32x4 a, u32x4 b) { return __any(max(mx4(a), mx4(b)) == SENT); }

DI void probe_parity(const Params& p) {
    if (threadIdx.x >= 64) return;
    const int l = threadIdx.x;
    const rsrc_t rs = __builtin_amdgcn_make_buffer_rsrc(p.ws, 0, (int)WS_END, 0x00020000);
    unsigned xcc; asm volatile("s_getreg_b32 %0, hwreg(HW_REG_XCC_ID)" : "=s"(xcc));
    const int po = (int)OFF_PROBE + (l & 7) * 8192 + 512;
    bst4(0u, rs, po, 0);
    DRAIN();
    int best0 = 1 << 30, best1 = 1 << 30;
#pragma unroll 1
    for (int k = 0; k < 8; ++k) {
        const int o = __builtin_amdgcn_readlane(po, k);
        unsigned v; unsigned long long c0, c1;
        asm volatile("s_memtime %0\n\ts_waitcnt lgkmcnt(0)\n\tbuffer_load_dword %2, %3, %4, 0 offen sc1\n\ts_waitcnt vmcnt(0)\n\ts_memtime %1\n\ts_waitcnt lgkmcnt(0)"
                     : "=&s"(c0), "=&s"(c1), "=&v"(v) : "v"(o), "s"(rs) : "memory");
        const int dt = (int)(c1 - c0);
        if (k & 1) best1 = min(best1, dt); else best0 = min(best0, dt);
    }
    const unsigned mine = best1 < best0 ? 1u : 0u;
    const unsigned par = ((xcc & 15) >= 4) ? 1u - mine : mine;
    if (l == 0) bst4(2u | par, rs, (int)OFF_CTRL, 0);
}
DI int wait_parity(rsrc_t rs) {
    unsigned v = 0;
#pragma unroll 1
    for (int att = 0; att < SPIN_CAP; ++att) {
        asm volatile("" ::: "memory");
        v = bld4(rs, (int)OFF_CTRL, 0);
        if (v & 2u) break;
        __builtin_amdgcn_s_sleep(4);
    }
    return __builtin_amdgcn_readfirstlane(v) & 1;
}

#define VLD16(dst, voff, soff) asm volatile("buffer_load_dwordx4 %0, %1, %2, %3 offen sc1" : "=v"(dst) : "v"(voff), "s"(rs), "s"(soff) : "memory")
#define VLD4(dst, voff, soff) asm volatile("buffer_load_dword %0, %1, %2, %3 offen sc1" : "=v"(dst) : "v"(voff), "s"(rs), "s"(soff) : "memory")
#define VST4(val, voff, soff) asm volatile("buffer_store_dword %0, %1, %2, %3 offen sc1" :: "v"(val), "v"(voff), "s"(rs), "s"(soff) : "memory")
#define VST8(val, voff, soff) asm volatile("buffer_store_dwordx2 %0, %1, %2, %3 offen sc1" :: "v"(val), "v"(voff), "s"(rs), "s"(soff) : "memory")
#define MM_AV0(d, a, b) asm volatile("v_mfma_f32_16x16x32_bf16 %0, %1, %2, 0" : "=&v"(d) : "a"(a), "v"(b))
#define MM_AV(d, a, b) asm volatile("v_mfma_f32_16x16x32_bf16 %0, %1, %2, %0" : "+v"(d) : "a"(a), "v"(b))
#define MM_VA0(d, a, b) asm volatile("v_mfma_f32_16x16x32_bf16 %0, %1, %2, 0" : "=&v"(d) : "v"(a), "a"(b))
#define MM_VA(d, a, b) asm volatile("v_mfma_f32_16x16x32_bf16 %0, %1, %2, %0" : "+v"(d) : "v"(a), "a"(b))
constexpr int OOB = 0x7FFFFF00;
#define REPL 0
#define FFN_DELAY 1
#define MODE 0
#define ADAPT_UP 0
#define ADAPT_DN 0
#define XUP 96
#define XDN 6
#define RAMP 64
#define RAMPN 10
#define LPT(k, idx, base)
#define VWAIT() do { SCHED(); asm volatile("s_waitcnt vmcnt(0)" ::: "memory"); SCHED(); } while (0)
template <int V> struct IntC { static constexpr int value = V; };

DI void chain_merged(const Params& p, int role, LAS unsigned char* L) {
    const int tid = threadIdx.x, l = tid & 63, w = __builtin_amdgcn_readfirstlane(tid >> 6), r16 = l & 15, q = l >> 4;
    const int h = role / 7, c = role % 7, g = role / 56, j = role % 56;
    const int parl = (l >> 3) & 1;
    const rsrc_t rs = __builtin_amdgcn_make_buffer_rsrc(p.ws, 0, (int)WS_END, 0x00020000);
    u32x4 AT[4][4], AZ[14], BT[2][14];
    {
#pragma unroll
        for (int tt = 0; tt < 4; ++tt)
#pragma unroll
            for (int kk = 0; kk < 4; ++kk) {
                const int ks = min(4 * w + kk, 13);
                AT[tt][kk] = gld16(p.ws + OFF_AT + piece(h, 2 * c + (tt >> 1), 4 * ks + q, 16 * (tt & 1) + r16));
            }
#pragma unroll
        for (int u = 0; u < 2; ++u)
#pragma unroll
            for (int ks = 0; ks < 14; ++ks) BT[u][ks] = gld16(p.ws + OFF_BT + piece(4 * w + 2 * u + parl, j >> 2, 4 * ks + q, 8 * (j & 3) + (l & 7)));
        SCHED();
#pragma unroll
        for (int tt = 0; tt < 4; ++tt)
#pragma unroll
            for (int kk = 0; kk < 4; ++kk) {
                if (4 * w + kk > 13) AT[tt][kk] = u32x4{0, 0, 0, 0};
                asm volatile("" : "+a"(AT[tt][kk]));
            }
    }
    const unsigned ONE = 0x3F80u;
    u32x4 SEL0, SEL1; u32x2 AONE, AW; f32x4 CINIT;
    {
#pragma unroll
        for (int n = 0; n < 4; ++n) { const unsigned v = (r16 == 2 * n ? ONE : 0u) | (r16 == 2 * n + 1 ? ONE << 16 : 0u); SEL0[n] = v; SEL1[n] = q < 3 ? v : 0u; }
        if (q == 3 && r16 == 8) { SEL1[0] = ONE | (ONE << 16); SEL1[1] = ONE; }
        AONE = u32x2{ONE | (ONE << 16), ONE | (ONE << 16)};
        AW = u32x2{q == 2 ? ONE : 0u, 0u};
        const float NI = -3.0e38f;
        CINIT = q < 2 ? f32x4{0, 0, 0, 0} : (q == 2 ? f32x4{0, NI, NI, NI} : f32x4{NI, NI, NI, NI});
    }
    if (tid < 64) *(LAS u32x4*)(L + L_ZERO + tid * 16) = u32x4{0, 0, 0, 0};
    const int ring = (int)OFF_RING + wait_parity(rs) * 8192;
    const int lo_m = (4 * parl + q) * 128 + (l & 7) * 16;
    const int vm0 = ring + min(2 * w, 6) * 1024 + lo_m, vm1 = w < 3 ? ring + (2 * w + 1) * 1024 + lo_m : OOB;
    const int vs0 = ring + SOFF + s_phys(((g * 4 + w) * 3 + 0) * 1024 + l * 16), vs1 = ring + SOFF + s_phys(((g * 4 + w) * 3 + 1) * 1024 + l * 16), vs2 = ring + SOFF + s_phys(((g * 4 + w) * 3 + 2) * 1024 + l * 16);
    const int vspub = ring + SOFF + (l & 1) * SCOPY + s_piece(h, c, l >> 3) + ((l & 7) >> 1) * 4;
    const int m4 = (l >> 3) & 3, f8 = l & 7;
    const bool toring = (f8 & 1) == 0;
    const int vmring = ring + (l >> 5) * MCOPY + j * 128 + (4 * g + m4) * 16 + (f8 >> 1) * 4;
    const int vmglob = (int)OFF_M + (ring - (int)OFF_RING) + j * 128 + (4 * g + m4) * 16 + (f8 >> 1) * 4;
    const int vra0 = vspub, vra1 = toring ? vmring : OOB;
    const int vu0 = (int)OFF_U + (((4 * w + parl) * 5 + q) * D + 8 * j + (l & 7)) * 16, vu1 = vu0 + 2 * 5 * D * 16;
    const int vy0 = l < 32 ? (int)OFF_U + (((4 * w + parl) * 5 + 4) * D + 8 * j + (l & 7)) * 16 + (q & 1) * 8 : OOB, vy1 = vy0 == OOB ? OOB : vy0 + 2 * 5 * D * 16;
    const int vz = parl ? OOB : (int)OFF_ZM + (h * 14 + 2 * c + (w >> 1)) * 512 + q * 128 + (l & 7) * 16 + (w & 1) * 8;
    const int vflag = (int)OFF_FLAG_ZU + (112 + ((4 * w + (l & 3)) * 14 + (j >> 2)) / 2) * 4;
    const LAS unsigned char* a0p = L + (parl ? L_ZERO : L_M + c * 1024) + l * 16;
    const LAS unsigned char* a1p = L + (parl ? L_M + c * 1024 : L_ZERO) + l * 16;
    const int hl = (l >> 2) & 3;
    const unsigned long long hvm = __builtin_amdgcn_ballot_w64((hl & 1) == (q >> 1)), hbm = __builtin_amdgcn_ballot_w64(hl >= 2), h1m = __builtin_amdgcn_ballot_w64((hl & 1) != 0);
    const unsigned SENTV = SENT;
    lds_barrier();
    unsigned tref = ct();
    unsigned slot = MODE == 1 ? (rt16() / p.tauX + 1) * p.tauX : 0;
    int tauM = p.tauM, tauS = p.tauS;
    u32x2 YA0 = {0, 0}, YA1 = {0, 0};
#define STAMP(i, from)
#define COUNT(i, v)
    const unsigned long long parm = __builtin_amdgcn_ballot_w64(parl != 0);
    LAS unsigned char* const ldm0 = L + L_M + (2 * w) * 1024 + l * 16;
    LAS unsigned char* const ldm1 = L + (w < 3 ? L_M + (2 * w + 1) * 1024 : L_DUMMY) + l * 16;
    const unsigned lds_sa = (unsigned)(size_t)(L + L_S + w * 256 + ((l & 7) * 8 + 4 * (q & 1) + 2 * parl + (l >> 5)) * 4);
    const unsigned lds_oa = (unsigned)(size_t)(L + L_O + w * 128 + ((l & 3) * 8 + 4 * (q & 1) + 2 * (hl & 1) + (l >> 5)) * 4);
    const unsigned a0a = (unsigned)(size_t)a0p, a1a = (unsigned)(size_t)a1p;
    const int mc0 = (REPL && ((role >> 2) & 1)) ? MCOPY : 0;
    u32x4 mf[7];
    const int nst = 1 + (g == 0 ? 2 : 0) + (w == 1 ? 2 : 0);
    u32x2 ZS = {0, 0};
    auto tail = [&]() {
        f32x4 ze, zo;
        asm volatile("v_mfma_f32_16x16x32_bf16 %0, %2, %4, 0\n\tv_mfma_f32_16x16x32_bf16 %1, %3, %4, 0" : "=&v"(ze), "=&v"(zo) : "a"(AZ[0]), "a"(AZ[1]), "v"(mf[0]));
#pragma unroll
        for (int i = 1; i < 7; ++i)
            asm volatile("v_mfma_f32_16x16x32_bf16 %0, %2, %4, %0\n\tv_mfma_f32_16x16x32_bf16 %1, %3, %4, %1" : "+v"(ze), "+v"(zo) : "a"(AZ[2 * i]), "a"(AZ[2 * i + 1]), "v"(mf[i]));
        asm volatile("s_nop 7" : "+v"(ze), "+v"(zo));
        float z[4];
#pragma unroll
        for (int r = 0; r < 4; ++r) z[r] = ze[r] + dpp<ROR8>(zo[r]);
        ZS = u32x2{pk2(z[0], z[1]), pk2(z[2], z[3])};
        asm volatile("" : "+v"(ZS));
    };
    auto stores = [&](int t) {
        const int vz_ = vz, vra0_ = vra0, vra1_ = vra1, vy0_ = vy0, vy1_ = vy1; const unsigned sent_ = SENTV;
        VST8(ZS, vz_, t * (16 * 14 * 512));
        if (g == 0) { VST8(YA0, vy0_, t * (16 * 5 * D * 16)); VST8(YA1, vy1_, t * (16 * 5 * D * 16)); }
        if (w == 1) { const int sp = ((t - 1) & 3) * SLOT; VST4(sent_, vra0_, sp); VST4(sent_, vra1_, sp); }
    };
    auto step = [&](int s, auto kindc) -> bool {
        constexpr bool LAST = decltype(kindc)::value == 2, FIRST = decltype(kindc)::value == 0;
        const int so = (s & 3) * SLOT, sU = s * (16 * 5 * D * 16);
        if (MODE == 1) wait_slot(slot); else if (!FIRST) wait_until(tref + tauM);
        STAMP(0, tref);
        u32x4 m0, m1;
        LPT(5, 0, slotA);
        VLD16(m0, vm0, so + mc0); VLD16(m1, vm1, so + mc0);
        VWAIT();
        LPT(2, 0, slotA);
        f32x4 te0, to0, te1, to1, te2, to2, te3, to3;
        unsigned chk;
#define T_HEAD() asm volatile( \
            "v_mfma_f32_16x16x32_bf16 %0, %5, %13, 0\n\tv_mfma_f32_16x16x32_bf16 %1, %6, %13, 0\n\t" \
            "v_max3_u32 %4, %15, %16, %17\n\t" \
            "v_mfma_f32_16x16x32_bf16 %2, %9, %13, 0\n\t" \
            "v_max3_u32 %4, %4, %18, %19\n\t" \
            "v_mfma_f32_16x16x32_bf16 %3, %10, %13, 0\n\t" \
            "v_max3_u32 %4, %4, %20, %21\n\t" \
            "v_mfma_f32_16x16x32_bf16 %0, %7, %14, %0\n\t" \
            "v_max_u32 %4, %4, %22\n\t" \
            "v_mfma_f32_16x16x32_bf16 %1, %8, %14, %1\n\tv_mfma_f32_16x16x32_bf16 %2, %11, %14, %2\n\tv_mfma_f32_16x16x32_bf16 %3, %12, %14, %3" \
            : "=&v"(te0), "=&v"(to0), "=&v"(te1), "=&v"(to1), "=&v"(chk) \
            : "a"(AT[0][0]), "a"(AT[0][1]), "a"(AT[0][2]), "a"(AT[0][3]), "a"(AT[1][0]), "a"(AT[1][1]), "a"(AT[1][2]), "a"(AT[1][3]), "v"(m0), "v"(m1), \
              "v"(m0.x), "v"(m0.y), "v"(m0.z), "v"(m0.w), "v"(m1.x), "v"(m1.y), "v"(m1.z), "v"(m1.w))
        T_HEAD();
        COUNT(8, 1);
        if (__any(chk == SENT)) {
#pragma unroll 1
            for (int att = 0;; ++att) {
                COUNT(8, 1);
                if (MODE == 1) { slot = next_slot(slot, p.tauX); wait_slot(slot); }
                asm volatile("buffer_load_dwordx4 %0, %2, %4, %5 offen sc1\n\tbuffer_load_dwordx4 %1, %3, %4, %5 offen sc1\n\ts_waitcnt vmcnt(0)" : "=&v"(m0), "=&v"(m1) : "v"(vm0), "v"(vm1), "s"(rs), "s"(so + MCOPY - mc0) : "memory");
                if (!bad2(m0, m1)) break;
                if (att > SPIN_CAP) { note_timeout(rs, 0x100 + s); return false; }
            }
            T_HEAD();
            if (MODE == 0) tauM += ADAPT_UP;
        }
        if (MODE == 0) tauM -= ADAPT_DN;
        slot += p.tauS + max(0, RAMPN - s) * RAMP;
        if (FIRST) {
#pragma unroll
            for (int ks = 0; ks < 14; ++ks) AZ[ks] = gld16(p.ws + OFF_A + piece(h, 2 * c + (w >> 1), 4 * ks + q, 16 * (w & 1) + r16));
            SCHED();
        }
        STAMP(1, ta);
        u32x4 U0, U1; unsigned flg;
        if (FIRST) {
#pragma unroll 1
            for (int att = 0;; ++att) {
                unsigned f2;
                asm volatile("buffer_load_dword %0, %1, %2, 0 offen sc1\n\ts_waitcnt vmcnt(0)" : "=&v"(f2) : "v"(vflag), "s"(rs) : "memory");
                if (!__any(f2 == 0)) break;
                if (att > SPIN_CAP) { note_timeout(rs, 0x200); return false; }
                __builtin_amdgcn_s_sleep(2);
            }
        }
        if (!LAST) { VLD16(U0, vu0, sU); VLD16(U1, vu1, sU); VLD4(flg, vflag, (s + 1) * 1024); }
        *(LAS u32x4*)ldm0 = m0;
        *(LAS u32x4*)ldm1 = m1;
        u32x4 B0;
        {
            unsigned c0, c1;
            asm volatile(
                "v_mfma_f32_16x16x32_bf16 %0, %[a20], %[m0], 0\n\tv_mfma_f32_16x16x32_bf16 %1, %[a21], %[m0], 0\n\t"
                "v_mfma_f32_16x16x32_bf16 %2, %[a30], %[m0], 0\n\tv_mfma_f32_16x16x32_bf16 %3, %[a31], %[m0], 0\n\t"
                "v_cndmask_b32 %8, %[e00], %[o00], %[pm]\n\tv_cndmask_b32 %9, %[e01], %[o01], %[pm]\n\t"
                "v_mfma_f32_16x16x32_bf16 %0, %[a22], %[m1], %0\n\t"
                "v_cvt_pk_bf16_f32 %4, %8, %9\n\tv_cndmask_b32 %8, %[e02], %[o02], %[pm]\n\tv_cndmask_b32 %9, %[e03], %[o03], %[pm]\n\t"
                "v_mfma_f32_16x16x32_bf16 %1, %[a23], %[m1], %1\n\t"
                "v_cvt_pk_bf16_f32 %5, %8, %9\n\tv_cndmask_b32 %8, %[e10], %[o10], %[pm]\n\tv_cndmask_b32 %9, %[e11], %[o11], %[pm]\n\t"
                "v_mfma_f32_16x16x32_bf16 %2, %[a32], %[m1], %2\n\t"
                "v_cvt_pk_bf16_f32 %6, %8, %9\n\tv_cndmask_b32 %8, %[e12], %[o12], %[pm]\n\tv_cndmask_b32 %9, %[e13], %[o13], %[pm]\n\t"
                "v_mfma_f32_16x16x32_bf16 %3, %[a33], %[m1], %3\n\t"
                "v_cvt_pk_bf16_f32 %7, %8, %9"
                : "=&v"(te2), "=&v"(to2), "=&v"(te3), "=&v"(to3), "=&v"(B0.x), "=&v"(B0.y), "=&v"(B0.z), "=&v"(B0.w), "=&v"(c0), "=&v"(c1)
                : [a20] "a"(AT[2][0]), [a21] "a"(AT[2][1]), [a22] "a"(AT[2][2]), [a23] "a"(AT[2][3]), [a30] "a"(AT[3][0]), [a31] "a"(AT[3][1]), [a32] "a"(AT[3][2]), [a33] "a"(AT[3][3]),
                  [m0] "v"(m0), [m1] "v"(m1), [pm] "s"(parm),
                  [e00] "v"(te0[0]), [e01] "v"(te0[1]), [e02] "v"(te0[2]), [e03] "v"(te0[3]), [o00] "v"(to0[0]), [o01] "v"(to0[1]), [o02] "v"(to0[2]), [o03] "v"(to0[3]),
                  [e10] "v"(te1[0]), [e11] "v"(te1[1]), [e12] "v"(te1[2]), [e13] "v"(te1[3]), [o10] "v"(to1[0]), [o11] "v"(to1[1]), [o12] "v"(to1[2]), [o13] "v"(to1[3]));
        }
        lds_barrier();
        LPT(3, 0, slotA);
        unsigned t2 = 0;
        if (!LAST) {
            asm volatile(
                "ds_read_b128 v[232:235], %[a0]\n\tds_read_b128 v[236:239], %[a1]\n\t"
                "s_nop 1\n\t"
                "v_cndmask_b32 v224, %[e20], %[o20], %[pm]\n\tv_cndmask_b32 v225, %[e21], %[o21], %[pm]\n\tv_cvt_pk_bf16_f32 v228, v224, v225\n\t"
                "v_cndmask_b32 v224, %[e22], %[o22], %[pm]\n\tv_cndmask_b32 v225, %[e23], %[o23], %[pm]\n\tv_cvt_pk_bf16_f32 v229, v224, v225\n\t"
                "v_cndmask_b32 v224, %[e30], %[o30], %[pm]\n\tv_cndmask_b32 v225, %[e31], %[o31], %[pm]\n\tv_cvt_pk_bf16_f32 v230, v224, v225\n\t"
                "v_cndmask_b32 v224, %[e32], %[o32], %[pm]\n\tv_cndmask_b32 v225, %[e33], %[o33], %[pm]\n\tv_cvt_pk_bf16_f32 v231, v224, v225\n\t"
                "s_waitcnt lgkmcnt(0)\n\t"
                "v_mfma_f32_16x16x32_bf16 v[240:243], v[232:235], %[b0], 0\n\t"
                "v_mfma_f32_16x16x32_bf16 v[240:243], v[236:239], v[228:231], v[240:243]\n\t"
                "s_nop 7\n\t"
                "v_add_f32_dpp v240, v240, v240 row_ror:8 row_mask:0xf bank_mask:0xf\n\tv_add_f32_dpp v241, v241, v241 row_ror:8 row_mask:0xf bank_mask:0xf\n\t"
                "v_add_f32_dpp v242, v242, v242 row_ror:8 row_mask:0xf bank_mask:0xf\n\tv_add_f32_dpp v243, v243, v243 row_ror:8 row_mask:0xf bank_mask:0xf\n\t"
                "v_permlane32_swap_b32 v240, v241\n\ts_nop 0\n\tv_permlane32_swap_b32 v242, v243\n\t"
                "v_add_f32 v240, v240, v241\n\tv_add_f32 v242, v242, v243\n\tv_cndmask_b32 v240, v240, v242, %[pm]\n\t"
                "ds_write_b32 %[dst], v240"
                :: [a0] "v"(a0a), [a1] "v"(a1a), [b0] "v"(B0), [pm] "s"(parm), [dst] "v"(lds_sa),
                   [e20] "v"(te2[0]), [e21] "v"(te2[1]), [e22] "v"(te2[2]), [e23] "v"(te2[3]), [o20] "v"(to2[0]), [o21] "v"(to2[1]), [o22] "v"(to2[2]), [o23] "v"(to2[3]),
                   [e30] "v"(te3[0]), [e31] "v"(te3[1]), [e32] "v"(te3[2]), [e33] "v"(te3[3]), [o30] "v"(to3[0]), [o31] "v"(to3[1]), [o32] "v"(to3[2]), [o33] "v"(to3[3])
                : "memory", "v224", "v225", "v228", "v229", "v230", "v231", "v232", "v233", "v234", "v235", "v236", "v237", "v238", "v239", "v240", "v241", "v242", "v243");
            if (MODE == 1) *(LAS unsigned*)(L + L_T + w * 4) = slot;
            lds_barrier();
            LPT(4, 0, slotA);
            t2 = ct();
            STAMP(2, ta);
            if (w == 0) {
                const LAS float* sp = (const LAS float*)(L + L_S) + l;
                const float v = (sp[0] + sp[64]) + (sp[128] + sp[192]);
                const unsigned pv = min(pk2(dpp<QP0022>(v), dpp<QP1133>(v)), SENT - 1);
                VST4(pv, vspub, so);
                LPT(1, 0, slotA);
            }
        }
        SCHED();
        if (MODE == 1 && !LAST) slot = slot_max(L + L_T);
        f32x4 ye0, yo0, ye1, yo1;
#pragma unroll
        for (int i = 0; i < 7; ++i) mf[i] = *(const LAS u32x4*)(L + L_M + i * 1024 + l * 16);
#define Y_STEP0(i) asm volatile("v_mfma_f32_16x16x32_bf16 %0, %4, %5, 0\n\tv_mfma_f32_16x16x32_bf16 %1, %4, %6, 0\n\tv_mfma_f32_16x16x32_bf16 %2, %4, %7, 0\n\tv_mfma_f32_16x16x32_bf16 %3, %4, %8, 0" \
            : "=&v"(ye0), "=&v"(yo0), "=&v"(ye1), "=&v"(yo1) : "v"(mf[i]), "a"(BT[0][2 * i]), "a"(BT[0][2 * i + 1]), "a"(BT[1][2 * i]), "a"(BT[1][2 * i + 1]))
#define Y_STEP(i) asm volatile("v_mfma_f32_16x16x32_bf16 %0, %4, %5, %0\n\tv_mfma_f32_16x16x32_bf16 %1, %4, %6, %1\n\tv_mfma_f32_16x16x32_bf16 %2, %4, %7, %2\n\tv_mfma_f32_16x16x32_bf16 %3, %4, %8, %3" \
            : "+v"(ye0), "+v"(yo0), "+v"(ye1), "+v"(yo1) : "v"(mf[i]), "a"(BT[0][2 * i]), "a"(BT[0][2 * i + 1]), "a"(BT[1][2 * i]), "a"(BT[1][2 * i + 1]))
        Y_STEP0(0); Y_STEP(1);
        SCHED();

        SCHED();
        Y_STEP(2);
        SCHED();
        u32x4 s0, s1, s2;
        if (!LAST) {
            STAMP(3, ta);
            if (MODE == 1) wait_slot(slot); else wait_until(t2 + tauS);
            STAMP(4, ta);
            LPT(5, 1, slotB);
            VLD16(s0, vs0, so); VLD16(s1, vs1, so); VLD16(s2, vs2, so);
        }
        SCHED();
        Y_STEP(3); Y_STEP(4); Y_STEP(5); Y_STEP(6);
        asm volatile("s_nop 7" : "+v"(ye0), "+v"(yo0), "+v"(ye1), "+v"(yo1));
        {
            float y[2][4];
#pragma unroll
            for (int r = 0; r < 4; ++r) {
                const auto x = __builtin_amdgcn_permlane32_swap(__float_as_uint(yo0[r]), __float_as_uint(yo0[r]), false, false); y[0][r] = ye0[r] + __uint_as_float(x[1]);
                const auto z = __builtin_amdgcn_permlane32_swap(__float_as_uint(yo1[r]), __float_as_uint(yo1[r]), false, false); y[1][r] = ye1[r] + __uint_as_float(z[1]);
            }
            YA0 = u32x2{pk2(y[0][0], y[0][1]), pk2(y[0][2], y[0][3])}; YA1 = u32x2{pk2(y[1][0], y[1][1]), pk2(y[1][2], y[1][3])};
            asm volatile("" : "+v"(YA0), "+v"(YA1));
        }
        if (LAST) return true;
        u32x2 YM0 = l < 32 ? YA0 : u32x2{0, 0}, YM1 = l < 32 ? YA1 : u32x2{0, 0};
        const int vmp = toring ? vmring + ((s + 1) & 3) * SLOT : vmglob + (s + 1) * 16384;
        asm volatile("" : "+v"(YM0), "+v"(YM1));
        SCHED();
        STAMP(5, ta);
        LPT(3, 1, slotB);
        VWAIT();
        LPT(2, 1, slotB);
        f32x4 dxa, dxb, ds;
#define B_HEAD() asm volatile( \
            "v_mfma_f32_16x16x32_bf16 %0, %4, %6, %10\n\tv_mfma_f32_16x16x32_bf16 %0, %5, %7, %0\n\t" \
            "v_max3_u32 %3, %12, %13, %14\n\tv_max3_u32 %3, %3, %15, %16\n\t" \
            "v_mfma_f32_16x16x32_bf16 %1, %8, %11, 0\n\t" \
            "v_max3_u32 %3, %3, %17, %18\n\tv_max3_u32 %3, %3, %19, %20\n\t" \
            "v_mfma_f32_16x16x32_bf16 %2, %9, %11, 0\n\t" \
            "v_max3_u32 %3, %3, %21, %22\n\tv_max_u32 %3, %3, %23" \
            : "=&v"(ds), "=&v"(dxa), "=&v"(dxb), "=&v"(chk) \
            : "v"(SEL0), "v"(SEL1), "v"(s0), "v"(s1), "v"(U0), "v"(U1), "v"(CINIT), "v"(s2), \
              "v"(s0.x), "v"(s0.y), "v"(s0.z), "v"(s0.w), "v"(s1.x), "v"(s1.y), "v"(s1.z), "v"(s1.w), "v"(s2.x), "v"(s2.y), "v"(s2.z), "v"(s2.w))
        B_HEAD();
        COUNT(9, 1);
        if (__any(chk == SENT)) {
#pragma unroll 1
            for (int att = 0;; ++att) {
                COUNT(9, 1);
                if (MODE == 1) { slot = next_slot(slot, p.tauX); wait_slot(slot); }
                asm volatile("buffer_load_dwordx4 %0, %3, %6, %7 offen sc1\n\tbuffer_load_dwordx4 %1, %4, %6, %7 offen sc1\n\tbuffer_load_dwordx4 %2, %5, %6, %7 offen sc1\n\ts_waitcnt vmcnt(0)"
                             : "=&v"(s0), "=&v"(s1), "=&v"(s2) : "v"(vs0), "v"(vs1), "v"(vs2), "s"(rs), "s"(so + SCOPY) : "memory");
                if (!__any(max(max(mx4(s0), mx4(s1)), mx4(s2)) == SENT)) break;
                if (att > SPIN_CAP) { note_timeout(rs, 0x300 + s); return false; }
            }
            B_HEAD();
            if (MODE == 0) tauS += ADAPT_UP;
        }
        if (MODE == 0) tauS -= ADAPT_DN;
        slot += p.tauM + max(0, RAMPN - s) * RAMP;
        STAMP(6, ta);
        asm volatile(
            "v_max3_f32 v224, %[d0], %[d1], %[d2]\n\t"
            "v_max_f32 v224, v224, %[d3]\n\t"
            "v_mov_b32 v225, v224\n\t"
            "s_nop 1\n\t"
            "v_permlane32_swap_b32 v224, v225\n\t"
            "v_max_f32 v224, v224, v225\n\t"
            "v_mov_b32 v225, v224\n\t"
            "s_nop 1\n\t"
            "v_permlane16_swap_b32 v224, v225\n\t"
            "v_max_f32 v224, v224, v225\n\t"
            "v_mul_f32 v224, 0xbdb8aa3b, v224\n\t"
            "v_fmamk_f32 v226, %[d0], 0x3db8aa3b, v224\n\t"
            "v_fmamk_f32 v227, %[d1], 0x3db8aa3b, v224\n\t"
            "v_exp_f32 v226, v226\n\t"
            "v_fmamk_f32 v228, %[d2], 0x3db8aa3b, v224\n\t"
            "v_exp_f32 v227, v227\n\t"
            "v_fmamk_f32 v229, %[d3], 0x3db8aa3b, v224\n\t"
            "v_exp_f32 v228, v228\n\t"
            "v_exp_f32 v229, v229\n\t"
            "v_cvt_pk_bf16_f32 v230, v226, v227\n\t"
            "v_cvt_pk_bf16_f32 v231, v228, v229\n\t"
            "v_cndmask_b32 v224, %[xa0], %[xb0], %[hb]\n\tv_cndmask_b32 v225, %[xa1], %[xb1], %[hb]\n\t"
            "v_mfma_f32_16x16x16_bf16 v[232:235], %[one], v[230:231], 0\n\t"
            "v_mfma_f32_16x16x16_bf16 v[236:239], %[aw], v[230:231], 0\n\t"
            "v_mfma_f32_16x16x16_bf16 v[240:243], %[y0], v[230:231], 0\n\t"
            "v_mfma_f32_16x16x16_bf16 v[244:247], %[y1], v[230:231], 0\n\t"
            "v_cndmask_b32 v226, %[xa2], %[xb2], %[hb]\n\tv_cndmask_b32 v227, %[xa3], %[xb3], %[hb]\n\t"
            "s_nop 2\n\t"
            "v_rcp_f32 v228, v232\n\t"
            "s_nop 0\n\t"
            "v_cndmask_b32 v228, 0, v228, %[hv]\n\t"
            "v_mul_f32 v229, v236, v228\n\t"
            "v_cndmask_b32 v240, v240, v244, %[hb]\n\tv_cndmask_b32 v241, v241, v245, %[hb]\n\t"
            "v_cndmask_b32 v242, v242, v246, %[hb]\n\tv_cndmask_b32 v243, v243, v247, %[hb]\n\t"
            "v_mul_f32 v240, v240, v228\n\tv_mul_f32 v241, v241, v228\n\tv_mul_f32 v242, v242, v228\n\tv_mul_f32 v243, v243, v228\n\t"
            "v_fmac_f32 v240, v224, v229\n\tv_fmac_f32 v241, v225, v229\n\tv_fmac_f32 v242, v226, v229\n\tv_fmac_f32 v243, v227, v229\n\t"
            "v_add_f32_dpp v240, v240, v240 row_ror:4 row_mask:0xf bank_mask:0xf\n\tv_add_f32_dpp v241, v241, v241 row_ror:4 row_mask:0xf bank_mask:0xf\n\t"
            "v_add_f32_dpp v242, v242, v242 row_ror:4 row_mask:0xf bank_mask:0xf\n\tv_add_f32_dpp v243, v243, v243 row_ror:4 row_mask:0xf bank_mask:0xf\n\t"
            "v_add_f32_dpp v240, v240, v240 row_ror:8 row_mask:0xf bank_mask:0xf\n\tv_add_f32_dpp v241, v241, v241 row_ror:8 row_mask:0xf bank_mask:0xf\n\t"
            "v_add_f32_dpp v242, v242, v242 row_ror:8 row_mask:0xf bank_mask:0xf\n\tv_add_f32_dpp v243, v243, v243 row_ror:8 row_mask:0xf bank_mask:0xf\n\t"
            "v_permlane32_swap_b32 v240, v241\n\ts_nop 0\n\tv_permlane32_swap_b32 v242, v243\n\t"
            "v_add_f32 v240, v240, v241\n\tv_add_f32 v242, v242, v243\n\tv_cndmask_b32 v240, v240, v242, %[h1]\n\t"
            "ds_write_b32 %[dst], v240"
            :: [d0] "v"(ds[0]), [d1] "v"(ds[1]), [d2] "v"(ds[2]), [d3] "v"(ds[3]), [one] "v"(AONE), [aw] "v"(AW), [y0] "v"(YM0), [y1] "v"(YM1),
               [hb] "s"(hbm), [hv] "s"(hvm), [h1] "s"(h1m), [dst] "v"(lds_oa),
               [xa0] "v"(dxa[0]), [xa1] "v"(dxa[1]), [xa2] "v"(dxa[2]), [xa3] "v"(dxa[3]), [xb0] "v"(dxb[0]), [xb1] "v"(dxb[1]), [xb2] "v"(dxb[2]), [xb3] "v"(dxb[3])
            : "memory", "v224", "v225", "v226", "v227", "v228", "v229", "v230", "v231", "v232", "v233", "v234", "v235", "v236", "v237", "v238", "v239",
              "v240", "v241", "v242", "v243", "v244", "v245", "v246", "v247");
        if (MODE == 1) *(LAS unsigned*)(L + L_T + 16 + w * 4) = slot;
        lds_barrier();
        LPT(4, 1, slotB);
        tref = ct();
        STAMP(7, ta);
        if (w == 0) {
            const LAS float* op = (const LAS float*)(L + L_O) + (l & 31);
            const float v = (op[0] + op[32]) + (op[64] + op[96]);
            const unsigned pv = min(pk2(dpp<QP0022>(v), dpp<QP1133>(v)), SENT - 1);
            VST4(pv, vmp, 0);
            LPT(1, 1, slotB);
        }
        SCHED();
        if (MODE == 1) slot = slot_max(L + L_T + 16);
        tail();
        SCHED();
        stores(s);
        if (__any(flg == 0)) {
#pragma unroll 1
            for (int att = 0;; ++att) {
                unsigned f2;
                asm volatile("buffer_load_dword %0, %1, %2, %3 offen sc1\n\ts_waitcnt vmcnt(0)" : "=&v"(f2) : "v"(vflag), "s"(rs), "s"((s + 1) * 1024) : "memory");
                if (!__any(f2 == 0)) break;
                if (att > SPIN_CAP) { note_timeout(rs, 0x200 + s); return false; }
                __builtin_amdgcn_s_sleep(2);
            }
        }
        return true;
    };
    if (!step(0, IntC<0>{})) return;
#pragma unroll
    for (int ks = 0; ks < 14; ++ks) asm volatile("" : "+a"(AZ[ks]));
#pragma unroll 1
    for (int s = 1; s < NSEG - 1; ++s) if (!step(s, IntC<1>{})) return;
    if (!step(NSEG - 1, IntC<2>{})) return;
    tail();
    stores(NSEG - 1);
    if (w == 0) { const unsigned z = 0; VST4(z, toring ? OOB : vmglob, NSEG * 16384); }
    DRAIN();
    lds_barrier();
    if (w == 0) { const unsigned z = 0; VST4(z, toring ? OOB : vmglob, (NSEG + 1) * 16384); }
    DRAIN();
}

DI void chain_mx(const Params& p, int h, LAS unsigned char* L) {
    const int tid = threadIdx.x, l = tid & 63, w = __builtin_amdgcn_readfirstlane(tid >> 6), q = l >> 4;
    const int parl = (l >> 3) & 1;
    const rsrc_t rs = __builtin_amdgcn_make_buffer_rsrc(p.ws, 0, (int)WS_END, 0x00020000);
    const int ring = (int)OFF_RING + wait_parity(rs) * 8192;
    const int lo_m = (4 * parl + q) * 128 + (l & 7) * 16;
    const int vm0 = ring + min(2 * w, 6) * 1024 + lo_m, vm1 = w < 3 ? ring + (2 * w + 1) * 1024 + lo_m : OOB;
    int vz[2][4];
#pragma unroll
    for (int kt = 0; kt < 2; ++kt)
#pragma unroll
        for (int kk = 0; kk < 4; ++kk) vz[kt][kk] = (int)OFF_Z + h * 28 * 1024 + l * 16 + (kt * 14 + min(4 * w + kk, 13)) * 1024;
    const int vflag = (int)OFF_FLAG_ZU + (h * 7 + min(l & 7, 6)) * 4;
    const int tok = 2 * w + (l >> 5), key = l & 31;
    const int vppub = ring + SOFF + (l & 1) * SCOPY + s_piece(h, 8 + (key >> 3), tok) + ((key & 7) >> 1) * 4;
    const int vlpub = key < 8 ? ring + SOFF + ((key >> 2) & 1) * SCOPY + s_piece(h, 7, tok) + (key & 3) * 4 : OOB;
    const unsigned long long parm = __builtin_amdgcn_ballot_w64(parl != 0), k0m = __builtin_amdgcn_ballot_w64((key & 3) == 0), k1m = __builtin_amdgcn_ballot_w64((key & 3) == 1);
    LAS unsigned char* const lds_w = L + L_X + w * 1024 + ((l & 7) * 32 + 16 * parl + 4 * q) * 4;
    const LAS float* const xp = (const LAS float*)(L + L_X) + tok * 32 + key;
    const unsigned SENTV = SENT;
    int tau = 0;
    unsigned slot = 0;
#define XSTAMP(i)
    int natt = 0;
    unsigned xs_z = 0, xs_f = 0;
    u32x4 Z00, Z01, Z02, Z03, Z10, Z11, Z12, Z13;
#define LOAD_Z(sz) do { VLD16(Z00, vz[0][0], sz); VLD16(Z01, vz[0][1], sz); VLD16(Z02, vz[0][2], sz); VLD16(Z03, vz[0][3], sz); \
                        VLD16(Z10, vz[1][0], sz); VLD16(Z11, vz[1][1], sz); VLD16(Z12, vz[1][2], sz); VLD16(Z13, vz[1][3], sz); } while (0)
#define WAIT_FLAG(sg) _Pragma("unroll 1") for (int att = 0;; ++att) { unsigned f2; asm volatile("buffer_load_dword %0, %1, %2, %3 offen sc1\n\ts_waitcnt vmcnt(0)" : "=&v"(f2) : "v"(vflag), "s"(rs), "s"((sg) * 1024) : "memory"); \
        if (!__any(f2 == 0)) break; if (att > SPIN_CAP) { note_timeout(rs, 0x400 + (sg)); return; } __builtin_amdgcn_s_sleep(2); }
    unsigned flg;
    WAIT_FLAG(0);
    LOAD_Z(0);
    VLD4(flg, vflag, 1024);
    tau = p.tauXm;
    unsigned tref = ct();
    for (int s = 0; s < NSEG - 1; ++s) {
        const int so = (s & 3) * SLOT;
        if (s > 0) wait_until(tref + tau);
        XSTAMP(0);
        u32x4 m0, m1;
        VLD16(m0, vm0, so); VLD16(m1, vm1, so);
        VWAIT();
        f32x4 te0, to0, te1, to1; unsigned chk;
#define X_HEAD() asm volatile( \
            "v_mfma_f32_16x16x32_bf16 %0, %5, %13, 0\n\tv_mfma_f32_16x16x32_bf16 %1, %6, %13, 0\n\t" \
            "v_max3_u32 %4, %15, %16, %17\n\t" \
            "v_mfma_f32_16x16x32_bf16 %2, %9, %13, 0\n\t" \
            "v_max3_u32 %4, %4, %18, %19\n\t" \
            "v_mfma_f32_16x16x32_bf16 %3, %10, %13, 0\n\t" \
            "v_max3_u32 %4, %4, %20, %21\n\t" \
            "v_mfma_f32_16x16x32_bf16 %0, %7, %14, %0\n\t" \
            "v_max_u32 %4, %4, %22\n\t" \
            "v_mfma_f32_16x16x32_bf16 %1, %8, %14, %1\n\tv_mfma_f32_16x16x32_bf16 %2, %11, %14, %2\n\tv_mfma_f32_16x16x32_bf16 %3, %12, %14, %3" \
            : "=&v"(te0), "=&v"(to0), "=&v"(te1), "=&v"(to1), "=&v"(chk) \
            : "v"(Z00), "v"(Z01), "v"(Z02), "v"(Z03), "v"(Z10), "v"(Z11), "v"(Z12), "v"(Z13), "v"(m0), "v"(m1), \
              "v"(m0.x), "v"(m0.y), "v"(m0.z), "v"(m0.w), "v"(m1.x), "v"(m1.y), "v"(m1.z), "v"(m1.w))
        X_HEAD();
        int att = 0;
        if (__any(chk == SENT)) {
#pragma unroll 1
            for (att = 1;; ++att) {
                asm volatile("buffer_load_dwordx4 %0, %2, %4, %5 offen sc1\n\tbuffer_load_dwordx4 %1, %3, %4, %5 offen sc1\n\ts_waitcnt vmcnt(0)" : "=&v"(m0), "=&v"(m1) : "v"(vm0), "v"(vm1), "s"(rs), "s"(so + MCOPY) : "memory");
                if (!bad2(m0, m1)) break;
                if (att > SPIN_CAP) { note_timeout(rs, 0x500 + s); return; }
            }
            X_HEAD();
            asm volatile("s_nop 1");
        }
        XSTAMP(1);
        f32x4 v;
        {
            float a0, a1, a2, a3, b0, b1, b2, b3;
            asm volatile(
                "s_nop 5\n\t"
                "v_cndmask_b32 %4, %[e10], %[o00], %[pm]\n\tv_cndmask_b32 %5, %[e11], %[o01], %[pm]\n\tv_cndmask_b32 %6, %[e12], %[o02], %[pm]\n\tv_cndmask_b32 %7, %[e13], %[o03], %[pm]\n\t"
                "v_cndmask_b32 %0, %[e00], %[o10], %[pm]\n\tv_cndmask_b32 %1, %[e01], %[o11], %[pm]\n\tv_cndmask_b32 %2, %[e02], %[o12], %[pm]\n\tv_cndmask_b32 %3, %[e03], %[o13], %[pm]\n\t"
                "v_add_f32_dpp %0, %4, %0 row_ror:8 row_mask:0xf bank_mask:0xf\n\tv_add_f32_dpp %1, %5, %1 row_ror:8 row_mask:0xf bank_mask:0xf\n\t"
                "v_add_f32_dpp %2, %6, %2 row_ror:8 row_mask:0xf bank_mask:0xf\n\tv_add_f32_dpp %3, %7, %3 row_ror:8 row_mask:0xf bank_mask:0xf"
                : "=&v"(a0), "=&v"(a1), "=&v"(a2), "=&v"(a3), "=&v"(b0), "=&v"(b1), "=&v"(b2), "=&v"(b3)
                : [pm] "s"(parm),
                  [e00] "v"(te0[0]), [e01] "v"(te0[1]), [e02] "v"(te0[2]), [e03] "v"(te0[3]), [o00] "v"(to0[0]), [o01] "v"(to0[1]), [o02] "v"(to0[2]), [o03] "v"(to0[3]),
                  [e10] "v"(te1[0]), [e11] "v"(te1[1]), [e12] "v"(te1[2]), [e13] "v"(te1[3]), [o10] "v"(to1[0]), [o11] "v"(to1[1]), [o12] "v"(to1[2]), [o13] "v"(to1[3]));
            v = f32x4{a0, a1, a2, a3};
        }
        *(LAS f32x4*)lds_w = v;
        lds_barrier();
        {
            const float sv = ((xp[0] + xp[256]) + (xp[512] + xp[768])) * SC2;
            float mx, t, e, sum, pn, lg;
            asm volatile(
                "s_nop 1\n\t"
                "v_max_f32_dpp %0, %4, %4 row_ror:1 row_mask:0xf bank_mask:0xf\n\ts_nop 1\n\t"
                "v_max_f32_dpp %0, %0, %0 row_ror:2 row_mask:0xf bank_mask:0xf\n\ts_nop 1\n\t"
                "v_max_f32_dpp %0, %0, %0 row_ror:4 row_mask:0xf bank_mask:0xf\n\ts_nop 1\n\t"
                "v_max_f32_dpp %0, %0, %0 row_ror:8 row_mask:0xf bank_mask:0xf\n\t"
                "v_mov_b32 %1, %0\n\ts_nop 1\n\t"
                "v_permlane16_swap_b32 %0, %1\n\t"
                "v_max_f32 %0, %0, %1\n\t"
                "v_sub_f32 %2, %4, %0\n\t"
                "v_exp_f32 %2, %2\n\ts_nop 1\n\t"
                "v_add_f32_dpp %3, %2, %2 row_ror:1 row_mask:0xf bank_mask:0xf\n\ts_nop 1\n\t"
                "v_add_f32_dpp %3, %3, %3 row_ror:2 row_mask:0xf bank_mask:0xf\n\ts_nop 1\n\t"
                "v_add_f32_dpp %3, %3, %3 row_ror:4 row_mask:0xf bank_mask:0xf\n\ts_nop 1\n\t"
                "v_add_f32_dpp %3, %3, %3 row_ror:8 row_mask:0xf bank_mask:0xf\n\t"
                "v_mov_b32 %1, %3\n\ts_nop 1\n\t"
                "v_permlane16_swap_b32 %3, %1\n\t"
                "v_add_f32 %3, %3, %1"
                : "=&v"(mx), "=&v"(t), "=&v"(e), "=&v"(sum) : "v"(sv));
            pn = e * __builtin_amdgcn_rcpf(sum);
            const unsigned pv = min(pk2(dpp<QP0022>(pn), dpp<QP1133>(pn)), SENT - 1);
            VST4(pv, vppub, so);
            lg = __builtin_amdgcn_logf(sum);
            const float lse = (mx + lg) * (1.0f / SC2);
            const float hi = __uint_as_float(pk2(lse, 0.f) << 16), r1 = lse - hi, mid = __uint_as_float(pk2(r1, 0.f) << 16), lo = r1 - mid;
            const unsigned d0 = pk2(hi, mid), d1 = pk2(lo, 0.f);
            const unsigned lv = min((key & 3) == 0 ? d0 : ((key & 3) == 1 ? d1 : 0u), SENT - 1);
            VST4(lv, vlpub, so);
        }
        XSTAMP(2);
        tref = ct();
        if (s > 2) tau = min(max(tau + (att == 0 ? -XDN : XUP), p.tauXm - 400), p.tauXm + 400);
        if (s >= 8) natt += att + 1;
        lds_barrier();
        __builtin_amdgcn_s_sleep(2);
        { const int sp = ((s - 1) & 3) * SLOT; VST4(SENTV, vppub, sp); VST4(SENTV, vlpub, sp); }
        if (__any(flg == 0)) { WAIT_FLAG(s + 1); }
        LOAD_Z((s + 1) * (16 * 28 * 1024));
        VLD4(flg, vflag, min(s + 2, NSEG - 1) * 1024);
    }
    DRAIN();
}

constexpr int XB = 32768;
DI void x_dma(const Params& p, int s, LAS unsigned char* buf, int w, int l) {
#pragma unroll
    for (int i = 0; i < 8; ++i) {
        const int r = 8 * w + i;
        int ch = l ^ (r & 15);
        ch = ch < 56 ? ch : ch - 8;
        __builtin_amdgcn_global_load_lds((const GAS unsigned*)(p.x + ((size_t)s * 32 + r) * D + ch * 8), (LAS unsigned*)(buf + r * 1024), 16, 0, 0);
    }
}
DI u32x4 swz_lo(u32x2& a, u32x2& b) {
    const auto x = __builtin_amdgcn_permlane32_swap(a.x, b.x, false, false);
    const auto y = __builtin_amdgcn_permlane32_swap(a.y, b.y, false, false);
    return u32x4{x[0], y[0], x[1], y[1]};
}
template <int NKS> DI void load_tr(u32x4 (&W)[NKS], const u16* src, int ld, LAS unsigned char* stg, int l) {
    constexpr int HALF = NKS / 2;
#pragma unroll
    for (int hf = 0; hf < 2; ++hf) {
        asm volatile("s_waitcnt lgkmcnt(0)" ::: "memory");
        for (int i = 0; i < HALF; ++i) {
            const int r = 16 * i + (l >> 2);
            *(LAS u32x4*)(stg + r * 64 + (l & 3) * 16) = gld16(src + (size_t)(hf * HALF * 16 + r) * ld + (l & 3) * 8);
        }
        asm volatile("s_waitcnt lgkmcnt(0)" ::: "memory");
#pragma unroll
        for (int k = 0; k < HALF; ++k) {
            const LAS unsigned char* a = stg + (16 * k + 8 * (l >> 5) + ((l & 15) >> 2)) * 64 + ((l >> 4) & 1) * 32 + (l & 3) * 8;
            const u32x2 x = __builtin_bit_cast(u32x2, __builtin_amdgcn_ds_read_tr16_b64_v4i16((LAS s16x4*)a));
            const u32x2 y = __builtin_bit_cast(u32x2, __builtin_amdgcn_ds_read_tr16_b64_v4i16((LAS s16x4*)(a + 4 * 64)));
            W[hf * HALF + k] = u32x4{x.x, x.y, y.x, y.y};
        }
    }
}

DI void x_dma1(const Params& p, int s, LAS unsigned char* buf, int r, int ch8) {
    __builtin_amdgcn_global_load_lds((const GAS unsigned*)(p.x + ((size_t)s * 32 + r) * D + ch8), (LAS unsigned*)(buf + r * 1024), 16, 0, 0);
}
template <int KIND>
DI void stream_x(const Params& p, int cu, LAS unsigned char* L) {
    const int tid = threadIdx.x, l = tid & 63, w = __builtin_amdgcn_readfirstlane(tid >> 6), hi = l >> 5, tk = l & 31;
    const rsrc_t rs = __builtin_amdgcn_make_buffer_rsrc(p.ws, 0, (int)WS_END, 0x00020000);
    const int wid = cu * 4 + w;
    u32x4 W[2][28];
    int h = 0, b = 0;
    x_dma(p, 0, L, w, l);
    if (KIND < 2) {
        const int T = wid * 2; h = T / 14; b = T % 14;
        const unsigned char* src = p.ws + (KIND == 0 ? OFF_A : OFF_BT);
#pragma unroll
        for (int ks = 0; ks < 28; ++ks)
#pragma unroll
            for (int t = 0; t < 2; ++t) W[t][ks] = gld16(src + piece(h, b + t, 2 * ks + hi, tk));
        SCHED();
    } else {
        LAS unsigned char* stg = L + 2 * XB + w * (14 * 16 * 64);
        load_tr<28>(W[0], p.Wg + 32 * wid, FF, stg, l);
        load_tr<28>(W[1], p.Wu + 32 * wid, FF, stg, l);
    }
    const int vflag = KIND == 2 ? (int)OFF_FLAG_H + wid * 4 : (int)OFF_FLAG_ZU + ((KIND == 1 ? 112 : 0) + wid) * 4;
    const int fstride = KIND == 2 ? 256 : 1024;
    constexpr int NST = KIND == 2 ? 2 : 4;
    const LAS unsigned char* xrow = L + tk * 1024;
    int xo[8], ch8[8];
#pragma unroll
    for (int k = 0; k < 8; ++k) { xo[k] = ((2 * k + hi) ^ (tk & 15)) * 16; int ch = l ^ ((8 * w + k) & 15); ch8[k] = (ch < 56 ? ch : ch - 8) * 8; }
    const int vst = KIND == 0 ? (int)OFF_Z + ((h * 2 + (tk >> 4)) * 14 + b) * 1024 + (hi * 16 + (tk & 15)) * 16
                  : KIND == 1 ? (int)OFF_U + ((h * 5 + hi) * D + 32 * b + tk) * 16 : (int)OFF_H + ((4 * wid + hi) * 32 + tk) * 16;
    constexpr int sstr = KIND == 0 ? 16 * 28 * 1024 : KIND == 1 ? 16 * 5 * D * 16 : 224 * 512;
    auto epi = [&](const f32x16 (&a)[2], int s, int i) {
        if (KIND == 0) {
            const int t = i >> 1, P = i & 1;
            const u32x4 v = {pk2(a[t][4 * P + 0], a[t][4 * P + 1]), pk2(a[t][4 * P + 2], a[t][4 * P + 3]), pk2(a[t][8 + 4 * P + 0], a[t][8 + 4 * P + 1]), pk2(a[t][8 + 4 * P + 2], a[t][8 + 4 * P + 3])};
            bst16(v, rs, vst + t * 1024 + P * 512, s * sstr);
        } else if (KIND == 1) {
            const int t = i >> 1, P = i & 1;
            u32x2 x = {pk2(a[t][8 * P + 0], a[t][8 * P + 1]), pk2(a[t][8 * P + 2], a[t][8 * P + 3])};
            u32x2 y = {pk2(a[t][8 * P + 4], a[t][8 * P + 5]), pk2(a[t][8 * P + 6], a[t][8 * P + 7])};
            const u32x4 v = swz_lo(x, y);
            bst16(v, rs, vst + t * 512 + P * (2 * D * 16), s * sstr);
        } else {
            float hv[8];
#pragma unroll
            for (int k = 0; k < 8; ++k) { const float gg = a[0][8 * i + k]; hv[k] = gg * __builtin_amdgcn_rcpf(1.f + __builtin_amdgcn_exp2f(-1.4426950408889634f * gg)) * a[1][8 * i + k]; }
            u32x2 x = {pk2(hv[0], hv[1]), pk2(hv[2], hv[3])}, y = {pk2(hv[4], hv[5]), pk2(hv[6], hv[7])};
            const u32x4 v = swz_lo(x, y);
            bst16(v, rs, vst + i * 1024, s * sstr);
        }
    };
    auto tile = [&](int s, f32x16 (&acc)[2], const f32x16 (&prev)[2], bool first = false) {
        if (first && KIND < 2) asm volatile("s_waitcnt vmcnt(56)" ::: "memory");
        else if (s < 4) { DRAIN(); if (s >= 2 && l == 0) bst4(1u, rs, vflag, (s - 2) * fstride); }
        else { asm volatile("s_waitcnt vmcnt(%0)" :: "n"(NST) : "memory"); if (l == 0) { bst4(1u, rs, vflag, (s - 3) * fstride); if (s == 4) bst4(1u, rs, vflag, 2 * fstride); } }
        lds_barrier();
        const LAS unsigned char* xb = xrow + (s & 1) * XB;
        LAS unsigned char* nb = L + ((s + 1) & 1) * XB;
        const int sn = min(s + 1, NSEG - 1);
        u32x4 xf[4];
#pragma unroll
        for (int k = 0; k < 3; ++k) xf[k] = *(const LAS u32x4*)(xb + xo[k]);
#pragma unroll
        for (int ks = 0; ks < 28; ++ks) {
            if (ks + 3 < 28) xf[(ks + 3) & 3] = *(const LAS u32x4*)(xb + xo[(ks + 3) & 7] + ((ks + 3) >> 3) * 256);
            SCHED();
            if (ks == 0) {
                if (KIND == 1) { mma32_wb<true>(acc[0], xf[0], W[0][0]); mma32_wb<true>(acc[1], xf[0], W[1][0]); }
                else { mma32_wa<true>(acc[0], W[0][0], xf[0]); mma32_wa<true>(acc[1], W[1][0], xf[0]); }
            } else {
                if (KIND == 1) { mma32_wb<false>(acc[0], xf[ks & 3], W[0][ks]); mma32_wb<false>(acc[1], xf[ks & 3], W[1][ks]); }
                else { mma32_wa<false>(acc[0], W[0][ks], xf[ks & 3]); mma32_wa<false>(acc[1], W[1][ks], xf[ks & 3]); }
            }
            SCHED();
            if (ks >= 1 && ks < 9) x_dma1(p, sn, nb, 8 * w + ks - 1, ch8[ks - 1]);
            if (ks >= 10 && (ks - 10) % (KIND == 2 ? 8 : 4) == 0 && (ks - 10) / (KIND == 2 ? 8 : 4) < NST && s > 1) epi(prev, s - 1, (ks - 10) / (KIND == 2 ? 8 : 4));
            SCHED();
        }
    };
    f32x16 accA[2], accB[2];
    tile(0, accB, accA, true);
    mma32_done(accB[0], accB[1]);
#pragma unroll
    for (int i = 0; i < NST; ++i) epi(accB, 0, i);
    DRAIN();
    if (l == 0) bst4(1u, rs, vflag, 0);
    tile(1, accB, accA);
#pragma unroll 1
    for (int s = 2; s < NSEG; s += 2) { tile(s, accA, accB); tile(s + 1, accB, accA); }
    mma32_done(accB[0], accB[1]);
#pragma unroll
    for (int i = 0; i < NST; ++i) epi(accB, NSEG - 1, i);
    DRAIN();
    if (l == 0) { bst4(1u, rs, vflag, (NSEG - 3) * fstride); bst4(1u, rs, vflag, (NSEG - 2) * fstride); bst4(1u, rs, vflag, (NSEG - 1) * fstride); }
}

DI void ffn_down(const Params& p, int cu14, LAS unsigned char* L) {
    const int cu = cu14 % 7, grp = cu14 / 7;
    const int tid = threadIdx.x, l = tid & 63, w = __builtin_amdgcn_readfirstlane(tid >> 6), hi = l >> 5, tk = l & 31;
    const rsrc_t rs = __builtin_amdgcn_make_buffer_rsrc(p.ws, 0, (int)WS_END, 0x00020000);
    u32x4 W[2][28];
    {
        LAS unsigned char* stg = L + w * (14 * 16 * 64);
        load_tr<28>(W[0], p.Wd + (size_t)(448 * w) * D + 64 * cu, D, stg, l);
        load_tr<28>(W[1], p.Wd + (size_t)(448 * w) * D + 64 * cu + 32, D, stg, l);
    }
    lds_barrier();
    const int vh = (int)OFF_H + (56 * w) * 512 + l * 16;
    const int vflag = (int)OFF_FLAG_H + (14 * w + min(l, 13)) * 4;
    LAS float* red = (LAS float*)L;
    float* fo = (float*)(p.ws + OFF_FO);
    auto wait_h = [&](int s) -> bool {
        for (int att = 0;; ++att) {
            asm volatile("" ::: "memory");
            const unsigned f = bld4(rs, vflag, s * 256);
            if (!__any(f == 0)) return true;
            if (att > SPIN_CAP) { note_timeout(rs, 0x600 + s); return false; }
            __builtin_amdgcn_s_sleep(8);
        }
    };
    if (!wait_h(grp)) return;
    u32x4 hf[28];
#pragma unroll
    for (int ks = 0; ks < 28; ++ks) hf[ks] = bld16(rs, vh + ks * 1024, grp * (224 * 512));
    for (int s = grp; s < NSEG; s += 2) {
        const int sn = min(s + 2, NSEG - 2 + grp);
        if (!wait_h(sn)) return;
        f32x16 acc[2];
#pragma unroll
        for (int ks = 0; ks < 28; ++ks) {
            if (ks == 0) { mma32_wb<true>(acc[0], hf[0], W[0][0]); mma32_wb<true>(acc[1], hf[0], W[1][0]); }
            else { mma32_wb<false>(acc[0], hf[ks], W[0][ks]); mma32_wb<false>(acc[1], hf[ks], W[1][ks]); }
            SCHED();
            hf[ks] = bld16(rs, vh + ks * 1024, sn * (224 * 512));
            SCHED();
        }
        mma32_done(acc[0], acc[1]);
        lds_barrier();
#pragma unroll
        for (int t = 0; t < 2; ++t)
#pragma unroll
            for (int i = 0; i < 16; ++i) red[((w * 2 + t) * 16 + i) * 64 + l] = acc[t][i];
        lds_barrier();
#pragma unroll
        for (int t = 0; t < 2; ++t)
#pragma unroll
            for (int r = 0; r < 4; ++r) {
                const int i = 4 * w + r;
                const float v = (red[((0 * 2 + t) * 16 + i) * 64 + l] + red[((1 * 2 + t) * 16 + i) * 64 + l]) + (red[((2 * 2 + t) * 16 + i) * 64 + l] + red[((3 * 2 + t) * 16 + i) * 64 + l]);
                bst4(__float_as_uint(v), rs, (int)OFF_FO + ((8 * w + 4 * hi + r) * D + 64 * cu + 32 * t + tk) * 4, s * (32 * D * 4));
            }
        DRAIN();
        if (l == 0) bst4(1u, rs, (int)OFF_FLAG_F + (cu * 4 + w) * 4, s * 128);
    }
}

DI bool wait_flags(rsrc_t rs, int voff, int soff, unsigned code) {
#pragma unroll 1
    for (int att = 0;; ++att) {
        asm volatile("" ::: "memory");
        const unsigned f = bld4(rs, voff, soff);
        if (!__any(f == 0)) return true;
        if (att > SPIN_CAP) { note_timeout(rs, code); return false; }
        __builtin_amdgcn_s_sleep(POLL_SLEEP);
    }
}
DI bool wait_data(rsrc_t rs, int voff, int soff, unsigned code) {
#pragma unroll 1
    for (int att = 0;; ++att) {
        asm volatile("" ::: "memory");
        const unsigned f = bld4(rs, voff, soff);
        if (!__any(f == SENT)) return true;
        if (att > SPIN_CAP) { note_timeout(rs, code); return false; }
        __builtin_amdgcn_s_sleep(POLL_SLEEP);
    }
}
DI void rows_role(const Params& p, int id, LAS unsigned char* L) {
    const int tid = threadIdx.x, l = tid & 63, w = __builtin_amdgcn_readfirstlane(tid >> 6), r16 = l & 15, q = l >> 4;
    const int grp = id >> 2, qi = id & 3;
    const rsrc_t rs = __builtin_amdgcn_make_buffer_rsrc(p.ws, 0, (int)WS_END, 0x00020000);
    LAS f32x4* red = (LAS f32x4*)(L + XB);
    LAS float* gate = (LAS float*)(L + XB + 4 * 14 * 1024);
    const int h = 4 * qi + w;
    const int k0 = 8 * (r16 >> 2) + (r16 & 3), k1 = k0 + 4;
    const int vz0 = (int)OFF_Z + h * 28 * 1024 + (k0 >> 4) * 14 * 1024 + (q * 16 + (k0 & 15)) * 16, vz1 = (int)OFF_Z + h * 28 * 1024 + (k1 >> 4) * 14 * 1024 + (q * 16 + (k1 & 15)) * 16;
    const int vzm = (int)OFF_ZM + h * 14 * 512 + q * 128 + (r16 & 7) * 16;
    const int mall = (int)OFF_M + wait_parity(rs) * 8192;
    for (int i = 0; i < PRETOUCH; ++i) __builtin_amdgcn_s_sleep(127);
#pragma unroll 1
    for (int sb = 0; sb < NSEG; sb += 16) {
        u32x4 t0[16], t1[16];
#pragma unroll
        for (int k16 = 0; k16 < 16; ++k16) {
            const int sg = sb + k16, u = (id * 4 + w + sg * 37) % 176;
            { const int hh = u / 14, k = u % 14; t0[k16] = bld16(rs, k < 7 ? (int)OFF_U + ((hh * 5 + 4) * D) * 16 + k * 1024 + l * 16 + sg * (16 * 5 * D * 16) : (int)OFF_ZM + hh * 7168 + (k - 7) * 1024 + l * 16 + sg * (16 * 14 * 512), 0); }
            { const int u2 = u + 176, hh = u2 / 14, k = u2 % 14; t1[k16] = bld16(rs, u >= 48 ? OOB : k < 7 ? (int)OFF_U + ((hh * 5 + 4) * D) * 16 + k * 1024 + l * 16 + sg * (16 * 5 * D * 16) : (int)OFF_ZM + hh * 7168 + (k - 7) * 1024 + l * 16 + sg * (16 * 14 * 512), 0); }
        }
#pragma unroll
        for (int k16 = 0; k16 < 16; ++k16) asm volatile("" :: "v"(t0[k16]), "v"(t1[k16]));
    }
#pragma unroll 1
    for (int s = grp; s < NSEG; s += 11) {
        lds_barrier();
        x_dma(p, s, L, w, l);
        if (!wait_flags(rs, (int)OFF_FLAG_ZU + (7 * h + min(l, 6)) * 4, s * 1024, 0x700 + s)) return;
        u32x4 z0[14], z1[14], zm[14];
#pragma unroll
        for (int ks = 0; ks < 14; ++ks) { z0[ks] = bld16(rs, vz0 + ks * 1024, s * (16 * 28 * 1024)); z1[ks] = bld16(rs, vz1 + ks * 1024, s * (16 * 28 * 1024)); }
        if (!wait_data(rs, mall + ((7 * h + min(l, 6)) % 56) * 128 + ((7 * h + min(l, 6)) / 56) * 64, (s + 2) * 16384, 0x800 + s)) return;
#pragma unroll
        for (int ks = 0; ks < 14; ++ks) zm[ks] = bld16(rs, vzm + ks * 512, s * (16 * 14 * 512));
        DRAIN();
        lds_barrier();
        f32x4 sc[3][2];
#pragma unroll
        for (int i = 0; i < 3; ++i) { sc[i][0] = f32x4{0, 0, 0, 0}; sc[i][1] = sc[i][0]; }
#pragma unroll
        for (int ks = 0; ks < 14; ++ks)
#pragma unroll
            for (int it = 0; it < 2; ++it) {
                const u32x4 xf = *(const LAS u32x4*)(L + (16 * it + r16) * 1024 + (((4 * ks + q) & 48) | (((4 * ks + q) & 15) ^ r16)) * 16);
                sc[0][it] = mfma16(z0[ks], xf, sc[0][it]); sc[1][it] = mfma16(z1[ks], xf, sc[1][it]); sc[2][it] = mfma16(zm[ks], xf, sc[2][it]);
            }
#pragma unroll
        for (int it = 0; it < 2; ++it) {
            const int i = 16 * it + r16;
            float v[12];
#pragma unroll
            for (int r = 0; r < 4; ++r) {
                v[r] = (8 * q + r <= i) ? sc[0][it][r] * SC2 : -3.0e38f;
                v[4 + r] = (8 * q + 4 + r <= i) ? sc[1][it][r] * SC2 : -3.0e38f;
                v[8 + r] = q < 2 ? sc[2][it][r] * SC2 : -3.0e38f;
            }
            float mx = v[0];
#pragma unroll
            for (int k = 1; k < 12; ++k) mx = fmaxf(mx, v[k]);
            mx = fmaxf(mx, __shfl_xor(mx, 16)); mx = fmaxf(mx, __shfl_xor(mx, 32));
            float sum = 0;
#pragma unroll
            for (int k = 0; k < 12; ++k) { v[k] = __builtin_amdgcn_exp2f(v[k] - mx); sum += v[k]; }
            sum += __shfl_xor(sum, 16); sum += __shfl_xor(sum, 32);
            const float rl = 1.0f / sum;
            const u32x4 px = {pk2(v[0] * rl, v[1] * rl), pk2(v[2] * rl, v[3] * rl), pk2(v[4] * rl, v[5] * rl), pk2(v[6] * rl, v[7] * rl)};
            bst16(px, rs, (int)OFF_PX + (h * 2 + it) * 1024 + l * 16, s * (16 * 2 * 1024));
            const u32x2 pm = {pk2(v[8] * rl, v[9] * rl), pk2(v[10] * rl, v[11] * rl)};
            bst8(pm, rs, (int)OFF_PM + (h * 2 + it) * 512 + l * 8, s * (16 * 2 * 512));
        }
        {
            const int row = tid >> 3, part = tid & 7;
            float g = 0;
#pragma unroll
            for (int k = 0; k < 7; ++k) {
                const int ch = part * 7 + k;
                const u32x4 a = *(const LAS u32x4*)(L + row * 1024 + ((ch & 48) | ((ch & 15) ^ (row & 15))) * 16), b = gld16(p.gw + ch * 8);
#pragma unroll
                for (int e = 0; e < 4; ++e) g += __uint_as_float(a[e] << 16) * __uint_as_float(b[e] << 16) + __uint_as_float(a[e] & 0xFFFF0000u) * __uint_as_float(b[e] & 0xFFFF0000u);
            }
            g += __shfl_xor(g, 1); g += __shfl_xor(g, 2); g += __shfl_xor(g, 4);
            if (part == 0) gate[row] = 1.0f / (1.0f + __expf(-g));
        }
        DRAIN();
        if (l == 0) bst4(1u, rs, (int)OFF_FLAG_P + (qi * 4 + w) * 4, s * 64);
        const int b0 = (112 * qi) >> 5, b1 = (112 * qi + 111) >> 5;
        if (!wait_flags(rs, (int)OFF_FLAG_ZU + (112 + ((4 * w + ((l >> 3) & 3)) * 14 + min(b0 + (l & 7), b1)) / 2) * 4, s * 1024, 0x900 + s)) return;
        u32x4 uf[4][7];
#pragma unroll
        for (int hh = 0; hh < 4; ++hh)
#pragma unroll
            for (int t = 0; t < 7; ++t) uf[hh][t] = bld16(rs, (int)OFF_U + (((4 * w + hh) * 5 + q) * D + 112 * qi + 16 * t + r16) * 16, s * (16 * 5 * D * 16));
        if (!wait_data(rs, mall + (14 * qi + min(l, 13)) * 128, (s + 2) * 16384, 0xA00 + s)) return;
        u32x2 yf[4][7];
#pragma unroll
        for (int hh = 0; hh < 4; ++hh)
#pragma unroll
            for (int t = 0; t < 7; ++t) yf[hh][t] = bld8(rs, q < 2 ? (int)OFF_U + (((4 * w + hh) * 5 + 4) * D + 112 * qi + 16 * t + r16) * 16 + q * 8 : OOB, s * (16 * 5 * D * 16));
        if (!wait_flags(rs, (int)OFF_FLAG_P + (l & 15) * 4, s * 64, 0xB00 + s)) return;
        f32x4 acc[7][2];
#pragma unroll
        for (int t = 0; t < 7; ++t) { acc[t][0] = f32x4{0, 0, 0, 0}; acc[t][1] = acc[t][0]; }
#pragma unroll
        for (int hh = 0; hh < 4; ++hh) {
            u32x4 px[2]; u32x2 pm[2];
#pragma unroll
            for (int it = 0; it < 2; ++it) {
                px[it] = bld16(rs, (int)OFF_PX + ((4 * w + hh) * 2 + it) * 1024 + l * 16, s * (16 * 2 * 1024));
                pm[it] = bld8(rs, (int)OFF_PM + ((4 * w + hh) * 2 + it) * 512 + l * 8, s * (16 * 2 * 512));
            }
#pragma unroll
            for (int t = 0; t < 7; ++t)
#pragma unroll
                for (int it = 0; it < 2; ++it) { acc[t][it] = mfma16(uf[hh][t], px[it], acc[t][it]); acc[t][it] = mfma16k16(yf[hh][t], pm[it], acc[t][it]); }
        }
#pragma unroll
        for (int t = 0; t < 7; ++t) { red[(w * 14 + 2 * t) * 64 + l] = acc[t][0]; red[(w * 14 + 2 * t + 1) * 64 + l] = acc[t][1]; }
        if (!wait_flags(rs, (int)OFF_FLAG_F + min(l, 27) * 4, s * 128, 0xC00 + s)) return;
        lds_barrier();
        for (int u = w; u < 14; u += 4) {
            const f32x4 a = (red[(0 * 14 + u) * 64 + l] + red[(1 * 14 + u) * 64 + l]) + (red[(2 * 14 + u) * 64 + l] + red[(3 * 14 + u) * 64 + l]);
            const int i = 16 * (u & 1) + r16, f = 112 * qi + 16 * (u >> 1) + 4 * q;
            const float g = gate[i];
            const u32x4 bb = bld16(rs, (int)OFF_FO + (i * D + f) * 4, s * (32 * D * 4));
            const u32x2 o = {pk2(g * a[0] + (1.f - g) * __uint_as_float(bb[0]), g * a[1] + (1.f - g) * __uint_as_float(bb[1])), pk2(g * a[2] + (1.f - g) * __uint_as_float(bb[2]), g * a[3] + (1.f - g) * __uint_as_float(bb[3]))};
            *(GAS u32x2*)(p.out + ((size_t)s * 32 + i) * D + f) = o;
        }
    }
}

__global__ void __launch_bounds__(256, 1) k_main(Params p) {
    extern __shared__ __attribute__((aligned(16))) unsigned char smem[];
    LAS unsigned char* L = (LAS unsigned char*)smem;
    const int b = blockIdx.x, xl = b & 7, idx = (b >> 3) * 4 + (xl & 3);
    if (xl < 4) {
        if (idx < 112) chain_merged(p, idx, L); else chain_mx(p, idx - 112, L);
    } else if (!(p.pad & 1) || idx >= 84) {
        if (idx < 28) stream_x<0>(p, idx, L);
        else if (idx < 56) stream_x<1>(p, idx - 28, L);
        else if (idx < 70) { for (int i = 0; i < FFN_DELAY; ++i) __builtin_amdgcn_s_sleep(127); stream_x<2>(p, idx - 56, L); }
        else if (idx < 84) { for (int i = 0; i < FFN_DELAY; ++i) __builtin_amdgcn_s_sleep(127); ffn_down(p, idx - 70, L); }
        else { if (idx == 127) probe_parity(p); if (!(p.pad & 3)) rows_role(p, idx - 84, L); }
    }
}

#define TAU_M 900
#define TAU_S 800
#define TAU_X 2700
int g_tauM = TAU_M, g_tauS = TAU_S, g_tauX = TAU_X, g_dbg = 0;

extern "C" void kernel_launch(void* const* d_in, const int* in_sizes, int n_in, void* d_out, int out_size, void* d_ws, size_t ws_size, hipStream_t stream) {
    Params p;
    p.x = (const u16*)d_in[0]; p.mem0 = (const u16*)d_in[1]; p.Wq = (const u16*)d_in[2]; p.Wk = (const u16*)d_in[3]; p.Wv = (const u16*)d_in[4]; p.Wo = (const u16*)d_in[5];
    p.Wg = (const u16*)d_in[8]; p.Wu = (const u16*)d_in[9]; p.Wd = (const u16*)d_in[10]; p.gw = (const u16*)d_in[11];
    p.ws = (unsigned char*)(((uintptr_t)d_ws + 2 * MB - 1) & ~(uintptr_t)(2 * MB - 1));
    p.out = (u16*)d_out;
    p.tauM = g_tauM; p.tauS = g_tauS; p.tauX = 384; p.tauXm = g_tauX; p.pad = g_dbg; p.pad2 = 0;
    constexpr int LDS_MAIN = 2 * XB + 4 * 14 * 16 * 64;
    hipFuncSetAttribute((const void*)k_main, hipFuncAttributeMaxDynamicSharedMemorySize, LDS_MAIN);
    hipFuncSetAttribute((const void*)k_prep, hipFuncAttributeMaxDynamicSharedMemorySize, 65536);
    k_prep<<<NPREP, 256, 65536, stream>>>(p);
    k_main<<<256, 256, LDS_MAIN, stream>>>(p);

}
```

```cpp
#include <hip/hip_runtime.h>
#include <stdint.h>

#define DI __device__ __forceinline__
#define LAS __attribute__((address_space(3)))
#define GAS __attribute__((address_space(1)))
typedef unsigned short u16;
typedef __bf16 bf16x8 __attribute__((ext_vector_type(8)));
typedef short s16x4 __attribute__((ext_vector_type(4)));
typedef float f32x4 __attribute__((ext_vector_type(4)));
typedef float f32x16 __attribute__((ext_vector_type(16)));
typedef unsigned u32x4 __attribute__((ext_vector_type(4)));
typedef unsigned u32x2 __attribute__((ext_vector_type(2)));
typedef __amdgpu_buffer_rsrc_t rsrc_t;

constexpr int D = 448, NH = 16, NSEG = 64, FF = 1792;
constexpr size_t MB = 1u << 20;
constexpr size_t OFF_RING = 0;
constexpr size_t OFF_M = 512 * 1024;
constexpr size_t OFF_PROBE = 1664 * 1024;
constexpr size_t OFF_CTRL = 2 * MB;
constexpr size_t OFF_FLAG_ZU = OFF_CTRL + 4096;
constexpr size_t OFF_FLAG_H = OFF_FLAG_ZU + 64 * 256 * 4;
constexpr size_t OFF_FLAG_F = OFF_FLAG_H + 64 * 64 * 4;
constexpr size_t OFF_FLAG_P = OFF_FLAG_F + 64 * 32 * 4;
constexpr size_t CTRL_END = OFF_FLAG_P + 64 * 16 * 4;
constexpr size_t OFF_STAT = 2 * MB + 512 * 1024;
constexpr size_t SZ_MAT = (size_t)NH * D * D * 2;
constexpr size_t OFF_A = 3 * MB;
constexpr size_t OFF_AT = OFF_A + 7 * MB;
constexpr size_t OFF_BT = OFF_AT + 7 * MB;
constexpr size_t OFF_Z = OFF_BT + 7 * MB;
constexpr size_t OFF_U = OFF_Z + 28 * MB;
constexpr size_t OFF_ZM = OFF_U + 35 * MB;
constexpr size_t OFF_H = OFF_ZM + 7 * MB;
constexpr size_t OFF_FO = OFF_H + 7 * MB;
constexpr size_t OFF_PX = OFF_FO + 4 * MB;
constexpr size_t OFF_PM = OFF_PX + 2 * MB;
constexpr size_t WS_END = OFF_PM + 1 * MB;
static_assert(WS_END + 2 * MB <= 256 * MB, "workspace");

constexpr int AUX_SC1 = 16;
constexpr unsigned SENT = 0xFFFFFFFFu;
constexpr float SC2 = 0.0625f * 1.4426950408889634f;

struct Params {
    const u16* x; const u16* mem0; const u16* Wq; const u16* Wk; const u16* Wv; const u16* Wo;
    const u16* Wg; const u16* Wu; const u16* Wd; const u16* gw;
    unsigned char* ws; u16* out;
    int tauM, tauS, tauX, tauXm, pad, pad2;
};

DI unsigned pk2(float a, float b) { typedef __bf16 bf2 __attribute__((ext_vector_type(2))); bf2 v = {(__bf16)a, (__bf16)b}; return __builtin_bit_cast(unsigned, v); }
DI f32x4 mfma16(u32x4 a, u32x4 b, f32x4 c) { return __builtin_amdgcn_mfma_f32_16x16x32_bf16(__builtin_bit_cast(bf16x8, a), __builtin_bit_cast(bf16x8, b), c, 0, 0, 0); }
DI f32x4 mfma16k16(u32x2 a, u32x2 b, f32x4 c) { return __builtin_amdgcn_mfma_f32_16x16x16bf16_1k(__builtin_bit_cast(s16x4, a), __builtin_bit_cast(s16x4, b), c, 0, 0, 0); }
DI f32x16 mfma32(u32x4 a, u32x4 b, f32x16 c) { return __builtin_amdgcn_mfma_f32_32x32x16_bf16(__builtin_bit_cast(bf16x8, a), __builtin_bit_cast(bf16x8, b), c, 0, 0, 0); }
template <bool FIRST> DI void mma32_wa(f32x16& acc, const u32x4& w, const u32x4& x) {
    if (FIRST) asm volatile("v_mfma_f32_32x32x16_bf16 %0, %1, %2, 0" : "=&v"(acc) : "a"(w), "v"(x));
    else asm volatile("v_mfma_f32_32x32x16_bf16 %0, %1, %2, %0" : "+v"(acc) : "a"(w), "v"(x));
}
template <bool FIRST> DI void mma32_wb(f32x16& acc, const u32x4& x, const u32x4& w) {
    if (FIRST) asm volatile("v_mfma_f32_32x32x16_bf16 %0, %1, %2, 0" : "=&v"(acc) : "v"(x), "a"(w));
    else asm volatile("v_mfma_f32_32x32x16_bf16 %0, %1, %2, %0" : "+v"(acc) : "v"(x), "a"(w));
}
DI void mma32_done(f32x16& a, f32x16& b) { asm volatile("s_nop 7\n\ts_nop 3" : "+v"(a), "+v"(b)); }
DI u32x4 gld16(const void* p) { return *(const GAS u32x4*)p; }
DI u32x4 bld16(rsrc_t r, int voff, int soff) { return __builtin_amdgcn_raw_buffer_load_b128(r, voff, soff, AUX_SC1); }
DI u32x2 bld8(rsrc_t r, int voff, int soff) { return __builtin_amdgcn_raw_buffer_load_b64(r, voff, soff, AUX_SC1); }
DI unsigned bld4(rsrc_t r, int voff, int soff) { return __builtin_amdgcn_raw_buffer_load_b32(r, voff, soff, AUX_SC1); }
DI void bst16(u32x4 v, rsrc_t r, int voff, int soff) { __builtin_amdgcn_raw_buffer_store_b128(v, r, voff + soff, 0, AUX_SC1); }
DI void bst8(u32x2 v, rsrc_t r, int voff, int soff) { __builtin_amdgcn_raw_buffer_store_b64(v, r, voff, soff, AUX_SC1); }
DI void bst4(unsigned v, rsrc_t r, int voff, int soff) { __builtin_amdgcn_raw_buffer_store_b32(v, r, voff, soff, AUX_SC1); }
DI unsigned ct() { return (unsigned)__builtin_amdgcn_s_memtime(); }
DI void wait_until(unsigned t) { while ((int)(ct() - t) < 0) {} }
DI unsigned rt16() { return (unsigned)__builtin_amdgcn_s_memrealtime() << 4; }
DI void wait_slot(unsigned slot) { while ((int)(rt16() - slot) < 0) {} }
DI unsigned slot_max(const LAS unsigned char* w) {
    const u32x4 v = *(const LAS u32x4*)w;
    const unsigned a = (int)(v.x - v.y) > 0 ? v.x : v.y, b = (int)(v.z - v.w) > 0 ? v.z : v.w;
    return __builtin_amdgcn_readfirstlane((int)(a - b) > 0 ? a : b);
}
DI unsigned next_slot(unsigned slot, unsigned q) { const unsigned now = rt16(); do slot += q; while ((int)(now - slot) >= 0); return slot; }
template <int CTRL> DI float dpp(float v) { return __int_as_float(__builtin_amdgcn_update_dpp(0, __float_as_int(v), CTRL, 0xf, 0xf, true)); }
constexpr int ROR1 = 0x121, ROR2 = 0x122, ROR4 = 0x124, ROR8 = 0x128, QP0022 = 0xA0, QP1133 = 0xF5;
DI unsigned mx4(u32x4 v) { return max(max(v.x, v.y), max(v.z, v.w)); }
DI void lds_barrier() { asm volatile("s_waitcnt lgkmcnt(0)" ::: "memory"); __builtin_amdgcn_s_barrier(); asm volatile("" ::: "memory"); }
#define SCHED() __builtin_amdgcn_sched_barrier(0)
#define DRAIN() asm volatile("s_waitcnt vmcnt(0)" ::: "memory")
constexpr int SPIN_CAP = 1 << 18;
#define PRETOUCH 1
#define POLL_SLEEP 32
DI void note_timeout(rsrc_t rs, unsigned code) { bst4(code, rs, (int)OFF_CTRL + 64, 0); }

DI int pidx(int i, int r) { return 32 * (i >> 1) + 8 * (r >> 2) + 4 * (i & 1) + (r & 3); }

constexpr int NPREP = 512;
#define PREP_SC1 1
#define WARM 1
DI size_t piece(int h, int blk, int oct, int pos) { return ((((size_t)h * 14 + blk) * 56 + oct) * 32 + pos) * 16; }

__global__ void __launch_bounds__(256, 2) k_prep(Params p) {
    extern __shared__ __attribute__((aligned(16))) unsigned char smem[];
    const int tid = threadIdx.x, l = tid & 63, w = __builtin_amdgcn_readfirstlane(tid >> 6), r16 = l & 15, q = l >> 4;
    const int blk = blockIdx.x;
#define PSTAMP(x)
    {
        if (w == 0) {
            const int i = blk * 64 + l, fb = i >> 10, o = i & 511;
            u32x4 v = {SENT, SENT, SENT, SENT};
            if (fb < 2 && o < 448) { const int b = o >> 3, t = o & 7; v = gld16(p.mem0 + t * D + 8 * b); v.x = min(v.x, SENT - 1); v.y = min(v.y, SENT - 1); v.z = min(v.z, SENT - 1); v.w = min(v.w, SENT - 1); }
            *(GAS u32x4*)(p.ws + OFF_RING + (size_t)i * 16) = v;
        } else if (w & 1) {
            constexpr int NW = (int)((CTRL_END - OFF_CTRL) / 4);
            static_assert(NW <= 2 * NPREP * 64, "flags");
            const int i = (blk * 2 + (w >> 1)) * 64 + l;
            if (i < NW && (!(p.pad & 1) || i >= (int)((OFF_FLAG_F - OFF_CTRL) / 4) || i < 16)) ((GAS unsigned*)(p.ws + OFF_CTRL))[i] = 0;
        } else if (w == 2) {
#pragma unroll
            for (int n = 0; n < 3; ++n) {
                const int i = (blk * 3 + n) * 64 + l, o = i & 511;
                u32x4 v = {SENT, SENT, SENT, SENT};
                if (i < 1024 && o < 448) v = gld16(p.mem0 + (o & 7) * D + 8 * (o >> 3));
                if (i < 67 * 1024) *(GAS u32x4*)(p.ws + OFF_M + (size_t)i * 16) = v;
            }
        }
        {
            u32x4 t[7];
#pragma unroll
            for (int n = 0; n < 7; ++n) {
                const int idx = blk * 256 + tid + n * (NPREP * 256), m = idx / 229376, o = idx % 229376;
                const u16* base = m == 0 ? p.Wq : m == 1 ? p.Wk : m == 2 ? p.Wv : p.Wo;
                t[n] = gld16(base + (size_t)o * 8);
            }
#pragma unroll
            for (int n = 0; n < 7; ++n) asm volatile("" :: "v"(t[n]));
        }
        PSTAMP(t1);
        const int T = blk >> 3;
        const bool tA = T < 32;
        const int h = 2 * (blk & 7) + ((T >> 4) & 1), tr = (T >> 2) & 3, tc = T & 3;
        const int wr = w >> 1, wc = w & 1, rt = 2 * tr + wr, ctl = 2 * tc + wc;
        const bool active = rt < 7 && ctl < 7;
        LAS unsigned char* L = (LAS unsigned char*)smem;
        const char* baseR = (const char*)(tA ? p.Wq : p.Wv); const char* baseC = (const char*)(tA ? p.Wk : p.Wo);
        unsigned offR[4], offC[4];
#pragma unroll
        for (int n = 0; n < 4; ++n) {
            const int rho = 8 * (4 * w + n) + (l >> 3), ch = (l & 7) ^ (rho & 7);
            const int rr = min(128 * tr + 64 * (rho >> 6) + pidx((rho & 63) >> 4, rho & 15), D - 1);
            offR[n] = ((rr * 16 + h) * 256 + ch * 8) * 2;
            if (tA) { const int cc = min(128 * tc + 64 * (rho >> 6) + pidx((rho & 63) >> 4, rho & 15), D - 1); offC[n] = ((cc * 16 + h) * 256 + ch * 8) * 2; }
            else { const int e = 4 * (4 * w + n) + (l >> 4), c16 = (l & 15) ^ ((e & 7) << 1); offC[n] = ((h * 256 + e) * D + min(128 * tc + 8 * c16, D - 8)) * 2; }
        }
        auto dma = [&](int kc) {
            LAS unsigned char* b = L + (kc & 1) * 32768;
            const char* bR = baseR + 128 * kc; const char* bC = baseC + (tA ? 128 * kc : 128 * kc * D);
#pragma unroll
            for (int n = 0; n < 4; ++n) {
                __builtin_amdgcn_global_load_lds((const GAS unsigned*)(bR + offR[n]), (LAS unsigned*)(b + (4 * w + n) * 1024), 16, 0, 0);
                __builtin_amdgcn_global_load_lds((const GAS unsigned*)(bC + offC[n]), (LAS unsigned*)(b + 16384 + (4 * w + n) * 1024), 16, 0, 0);
            }
        };
        f32x4 a1[4][4], a2[4][4];
#pragma unroll
        for (int i = 0; i < 4; ++i)
#pragma unroll
            for (int j = 0; j < 4; ++j) { a1[i][j] = f32x4{0, 0, 0, 0}; a2[i][j] = a1[i][j]; }
        dma(0); dma(1);
#pragma unroll 1
        for (int kc = 0; kc < 4; ++kc) {
            if (kc < 3) asm volatile("s_waitcnt vmcnt(8)" ::: "memory"); else DRAIN();
            lds_barrier();
            const LAS unsigned char* b = L + (kc & 1) * 32768;
            if (active) {
#pragma unroll
                for (int k2 = 0; k2 < 2; ++k2) {
                    u32x4 fa[4], fb[4];
#pragma unroll
                    for (int i = 0; i < 4; ++i) { const int rho = 64 * wr + 16 * i + r16; fa[i] = *(const LAS u32x4*)(b + rho * 128 + ((4 * k2 + q) ^ (rho & 7)) * 16); }
                    if (tA) {
#pragma unroll
                        for (int j = 0; j < 4; ++j) { const int rho = 64 * wc + 16 * j + r16; fb[j] = *(const LAS u32x4*)(b + 16384 + rho * 128 + ((4 * k2 + q) ^ (rho & 7)) * 16); }
#pragma unroll
                        for (int i = 0; i < 4; ++i)
#pragma unroll
                            for (int j = 0; j < 4; ++j) { a1[i][j] = mfma16(fa[i], fb[j], a1[i][j]); a2[j][i] = mfma16(fb[j], fa[i], a2[j][i]); }
                    } else {
#pragma unroll
                        for (int j = 0; j < 4; ++j) {
                            const int e0 = 32 * k2 + 8 * q + (r16 >> 2), c16 = 8 * wc + 2 * j + ((l & 3) >> 1);
                            const s16x4 lo = __builtin_amdgcn_ds_read_tr16_b64_v4i16((LAS s16x4*)(b + 16384 + e0 * 256 + (c16 ^ ((e0 & 7) << 1)) * 16 + (l & 1) * 8));
                            const s16x4 hi = __builtin_amdgcn_ds_read_tr16_b64_v4i16((LAS s16x4*)(b + 16384 + (e0 + 4) * 256 + (c16 ^ (((e0 + 4) & 7) << 1)) * 16 + (l & 1) * 8));
                            const u32x2 x = __builtin_bit_cast(u32x2, lo), y = __builtin_bit_cast(u32x2, hi);
                            fb[j] = u32x4{x.x, x.y, y.x, y.y};
                        }
#pragma unroll
                        for (int i = 0; i < 4; ++i)
#pragma unroll
                            for (int j = 0; j < 4; ++j) a1[i][j] = mfma16(fa[i], fb[j], a1[i][j]);
                    }
                }
            }
            if (kc < 2) { lds_barrier(); dma(kc + 2); }
        }
        PSTAMP(t2);
        const rsrc_t rs = __builtin_amdgcn_make_buffer_rsrc(p.ws, 0, (int)WS_END, 0x00020000);
#define PST(v, off) bst16(v, rs, (int)(off), 0)
        if (active) {
#pragma unroll
            for (int P = 0; P < 2; ++P)
#pragma unroll
                for (int j = 0; j < 4; ++j) {
                    const u32x4 v1 = {pk2(a1[2 * P][j][0], a1[2 * P][j][1]), pk2(a1[2 * P][j][2], a1[2 * P][j][3]), pk2(a1[2 * P + 1][j][0], a1[2 * P + 1][j][1]), pk2(a1[2 * P + 1][j][2], a1[2 * P + 1][j][3])};
                    PST(v1, (tA ? OFF_AT : OFF_BT) + piece(h, 2 * ctl + (j >> 1), 8 * rt + 4 * P + q, 16 * (j & 1) + r16));
                    if (tA) {
                        const u32x4 v2 = {pk2(a2[2 * P][j][0], a2[2 * P][j][1]), pk2(a2[2 * P][j][2], a2[2 * P][j][3]), pk2(a2[2 * P + 1][j][0], a2[2 * P + 1][j][1]), pk2(a2[2 * P + 1][j][2], a2[2 * P + 1][j][3])};
                        PST(v2, OFF_A + piece(h, 2 * rt + (j >> 1), 8 * ctl + 4 * P + q, 16 * (j & 1) + r16));
                    }
                }
        }
    }
}

DI int s_phys(int o) { return (o >> 13) * 16384 + (o & 8191); }
DI int s_piece(int h, int pc, int m) { return s_phys((((m >> 2) * 4 + (h >> 2)) * 3 + (pc >> 2)) * 1024 + (pc & 3) * 256 + (h & 3) * 64 + (m & 3) * 16); }
constexpr int SLOT = 8 * 16384, MCOPY = 16384, SOFF = 2 * 16384, SCOPY = 3 * 16384;
constexpr int L_M = 0;
constexpr int L_ZERO = 7168;
constexpr int L_S = 8192;
constexpr int L_O = 9216;
constexpr int L_X = 10240;
constexpr int L_DUMMY = 14336;
constexpr int L_T = 15360;

DI bool bad2(u32x4 a, u32x4 b) { return __any(max(mx4(a), mx4(b)) == SENT); }

DI void probe_parity(const Params& p) {
    if (threadIdx.x >= 64) return;
    const int l = threadIdx.x;
    const rsrc_t rs = __builtin_amdgcn_make_buffer_rsrc(p.ws, 0, (int)WS_END, 0x00020000);
    unsigned xcc; asm volatile("s_getreg_b32 %0, hwreg(HW_REG_XCC_ID)" : "=s"(xcc));
    const int po = (int)OFF_PROBE + (l & 7) * 8192 + 512;
    bst4(0u, rs, po, 0);
    DRAIN();
    int best0 = 1 << 30, best1 = 1 << 30;
#pragma unroll 1
    for (int k = 0; k < 8; ++k) {
        const int o = __builtin_amdgcn_readlane(po, k);
        unsigned v; unsigned long long c0, c1;
        asm volatile("s_memtime %0\n\ts_waitcnt lgkmcnt(0)\n\tbuffer_load_dword %2, %3, %4, 0 offen sc1\n\ts_waitcnt vmcnt(0)\n\ts_memtime %1\n\ts_waitcnt lgkmcnt(0)"
                     : "=&s"(c0), "=&s"(c1), "=&v"(v) : "v"(o), "s"(rs) : "memory");
        const int dt = (int)(c1 - c0);
        if (k & 1) best1 = min(best1, dt); else best0 = min(best0, dt);
    }
    const unsigned mine = best1 < best0 ? 1u : 0u;
    const unsigned par = ((xcc & 15) >= 4) ? 1u - mine : mine;
    if (l == 0) bst4(2u | par, rs, (int)OFF_CTRL, 0);
}
DI int wait_parity(rsrc_t rs) {
    unsigned v = 0;
#pragma unroll 1
    for (int att = 0; att < SPIN_CAP; ++att) {
        asm volatile("" ::: "memory");
        v = bld4(rs, (int)OFF_CTRL, 0);
        if (v & 2u) break;
        __builtin_amdgcn_s_sleep(4);
    }
    return __builtin_amdgcn_readfirstlane(v) & 1;
}

#define VLD16(dst, voff, soff) asm volatile("buffer_load_dwordx4 %0, %1, %2, %3 offen sc1" : "=v"(dst) : "v"(voff), "s"(rs), "s"(soff) : "memory")
#define VLD4(dst, voff, soff) asm volatile("buffer_load_dword %0, %1, %2, %3 offen sc1" : "=v"(dst) : "v"(voff), "s"(rs), "s"(soff) : "memory")
#define VST4(val, voff, soff) asm volatile("buffer_store_dword %0, %1, %2, %3 offen sc1" :: "v"(val), "v"(voff), "s"(rs), "s"(soff) : "memory")
#define VST8(val, voff, soff) asm volatile("buffer_store_dwordx2 %0, %1, %2, %3 offen sc1" :: "v"(val), "v"(voff), "s"(rs), "s"(soff) : "memory")
#define MM_AV0(d, a, b) asm volatile("v_mfma_f32_16x16x32_bf16 %0, %1, %2, 0" : "=&v"(d) : "a"(a), "v"(b))
#define MM_AV(d, a, b) asm volatile("v_mfma_f32_16x16x32_bf16 %0, %1, %2, %0" : "+v"(d) : "a"(a), "v"(b))
#define MM_VA0(d, a, b) asm volatile("v_mfma_f32_16x16x32_bf16 %0, %1, %2, 0" : "=&v"(d) : "v"(a), "a"(b))
#define MM_VA(d, a, b) asm volatile("v_mfma_f32_16x16x32_bf16 %0, %1, %2, %0" : "+v"(d) : "v"(a), "a"(b))
constexpr int OOB = 0x7FFFFF00;
#define REPL 3
#define FFN_DELAY 1
#define MODE 0
#define ADAPT_UP 0
#define ADAPT_DN 0
#define XUP 96
#define XDN 6
#define RAMP 64
#define RAMPN 10
#define LPT(k, idx, base)
#define VWAIT() do { SCHED(); asm volatile("s_waitcnt vmcnt(0)" ::: "memory"); SCHED(); } while (0)
template <int V> struct IntC { static constexpr int value = V; };

DI void chain_merged(const Params& p, int role, LAS unsigned char* L) {
    const int tid = threadIdx.x, l = tid & 63, w = __builtin_amdgcn_readfirstlane(tid >> 6), r16 = l & 15, q = l >> 4;
    const int h = role / 7, c = role % 7, g = role / 56, j = role % 56;
    const int parl = (l >> 3) & 1;
    const rsrc_t rs = __builtin_amdgcn_make_buffer_rsrc(p.ws, 0, (int)WS_END, 0x00020000);
    u32x4 AT[4][4], AZ[14], BT[2][14];
    {
#pragma unroll
        for (int tt = 0; tt < 4; ++tt)
#pragma unroll
            for (int kk = 0; kk < 4; ++kk) {
                const int ks = min(4 * w + kk, 13);
                AT[tt][kk] = gld16(p.ws + OFF_AT + piece(h, 2 * c + (tt >> 1), 4 * ks + q, 16 * (tt & 1) + r16));
            }
#pragma unroll
        for (int u = 0; u < 2; ++u)
#pragma unroll
            for (int ks = 0; ks < 14; ++ks) BT[u][ks] = gld16(p.ws + OFF_BT + piece(4 * w + 2 * u + parl, j >> 2, 4 * ks + q, 8 * (j & 3) + (l & 7)));
        SCHED();
#pragma unroll
        for (int tt = 0; tt < 4; ++tt)
#pragma unroll
            for (int kk = 0; kk < 4; ++kk) {
                if (4 * w + kk > 13) AT[tt][kk] = u32x4{0, 0, 0, 0};
                asm volatile("" : "+a"(AT[tt][kk]));
            }
    }
    const unsigned ONE = 0x3F80u;
    u32x4 SEL0, SEL1; u32x2 AONE, AW; f32x4 CINIT;
    {
#pragma unroll
        for (int n = 0; n < 4; ++n) { const unsigned v = (r16 == 2 * n ? ONE : 0u) | (r16 == 2 * n + 1 ? ONE << 16 : 0u); SEL0[n] = v; SEL1[n] = q < 3 ? v : 0u; }
        if (q == 3 && r16 == 8) { SEL1[0] = ONE | (ONE << 16); SEL1[1] = ONE; }
        AONE = u32x2{ONE | (ONE << 16), ONE | (ONE << 16)};
        AW = u32x2{q == 2 ? ONE : 0u, 0u};
        const float NI = -3.0e38f;
        CINIT = q < 2 ? f32x4{0, 0, 0, 0} : (q == 2 ? f32x4{0, NI, NI, NI} : f32x4{NI, NI, NI, NI});
    }
    if (tid < 64) *(LAS u32x4*)(L + L_ZERO + tid * 16) = u32x4{0, 0, 0, 0};
    const int ring = (int)OFF_RING + wait_parity(rs) * 8192;
    const int lo_m = (4 * parl + q) * 128 + (l & 7) * 16;
    const int vm0 = ring + min(2 * w, 6) * 1024 + lo_m, vm1 = w < 3 ? ring + (2 * w + 1) * 1024 + lo_m : OOB;
    const int vs0 = ring + SOFF + s_phys(((g * 4 + w) * 3 + 0) * 1024 + l * 16), vs1 = ring + SOFF + s_phys(((g * 4 + w) * 3 + 1) * 1024 + l * 16), vs2 = ring + SOFF + s_phys(((g * 4 + w) * 3 + 2) * 1024 + l * 16);
    const int vspub = ring + SOFF + (l & 1) * SCOPY + s_piece(h, c, l >> 3) + ((l & 7) >> 1) * 4;
    const int m4 = (l >> 3) & 3, f8 = l & 7;
    const bool toring = (f8 & 1) == 0;
    const int vmring = ring + (l >> 5) * MCOPY + j * 128 + (4 * g + m4) * 16 + (f8 >> 1) * 4;
    const int vmglob = (int)OFF_M + (ring - (int)OFF_RING) + j * 128 + (4 * g + m4) * 16 + (f8 >> 1) * 4;
    const int vra0 = vspub, vra1 = toring ? vmring : OOB;
    const int vu0 = (int)OFF_U + (((4 * w + parl) * 5 + q) * D + 8 * j + (l & 7)) * 16, vu1 = vu0 + 2 * 5 * D * 16;
    const int vy0 = l < 32 ? (int)OFF_U + (((4 * w + parl) * 5 + 4) * D + 8 * j + (l & 7)) * 16 + (q & 1) * 8 : OOB, vy1 = vy0 == OOB ? OOB : vy0 + 2 * 5 * D * 16;
    const int vz = parl ? OOB : (int)OFF_ZM + (h * 14 + 2 * c + (w >> 1)) * 512 + q * 128 + (l & 7) * 16 + (w & 1) * 8;
    const int vflag = (int)OFF_FLAG_ZU + (112 + ((4 * w + (l & 3)) * 14 + (j >> 2)) / 2) * 4;
    const LAS unsigned char* a0p = L + (parl ? L_ZERO : L_M + c * 1024) + l * 16;
    const LAS unsigned char* a1p = L + (parl ? L_M + c * 1024 : L_ZERO) + l * 16;
    const int hl = (l >> 2) & 3;
    const unsigned long long hvm = __builtin_amdgcn_ballot_w64((hl & 1) == (q >> 1)), hbm = __builtin_amdgcn_ballot_w64(hl >= 2), h1m = __builtin_amdgcn_ballot_w64((hl & 1) != 0);
    const unsigned SENTV = SENT;
    lds_barrier();
    unsigned tref = ct();
    unsigned slot = MODE == 1 ? (rt16() / p.tauX + 1) * p.tauX : 0;
    int tauM = p.tauM, tauS = p.tauS;
    u32x2 YA0 = {0, 0}, YA1 = {0, 0};
#define STAMP(i, from)
#define COUNT(i, v)
    const unsigned long long parm = __builtin_amdgcn_ballot_w64(parl != 0);
    LAS unsigned char* const ldm0 = L + L_M + (2 * w) * 1024 + l * 16;
    LAS unsigned char* const ldm1 = L + (w < 3 ? L_M + (2 * w + 1) * 1024 : L_DUMMY) + l * 16;
    const unsigned lds_sa = (unsigned)(size_t)(L + L_S + w * 256 + ((l & 7) * 8 + 4 * (q & 1) + 2 * parl + (l >> 5)) * 4);
    const unsigned lds_oa = (unsigned)(size_t)(L + L_O + w * 128 + ((l & 3) * 8 + 4 * (q & 1) + 2 * (hl & 1) + (l >> 5)) * 4);
    const unsigned a0a = (unsigned)(size_t)a0p, a1a = (unsigned)(size_t)a1p;
    const int mc0 = ((REPL & 1) && ((role >> 2) & 1)) ? MCOPY : 0, sc0 = ((REPL & 2) && ((role >> 2) & 1)) ? SCOPY : 0;
    u32x4 mf[7];
    const int nst = 1 + (g == 0 ? 2 : 0) + (w == 1 ? 2 : 0);
    u32x2 ZS = {0, 0};
    auto tail = [&]() {
        f32x4 ze, zo;
        asm volatile("v_mfma_f32_16x16x32_bf16 %0, %2, %4, 0\n\tv_mfma_f32_16x16x32_bf16 %1, %3, %4, 0" : "=&v"(ze), "=&v"(zo) : "a"(AZ[0]), "a"(AZ[1]), "v"(mf[0]));
#pragma unroll
        for (int i = 1; i < 7; ++i)
            asm volatile("v_mfma_f32_16x16x32_bf16 %0, %2, %4, %0\n\tv_mfma_f32_16x16x32_bf16 %1, %3, %4, %1" : "+v"(ze), "+v"(zo) : "a"(AZ[2 * i]), "a"(AZ[2 * i + 1]), "v"(mf[i]));
        asm volatile("s_nop 7" : "+v"(ze), "+v"(zo));
        float z[4];
#pragma unroll
        for (int r = 0; r < 4; ++r) z[r] = ze[r] + dpp<ROR8>(zo[r]);
        ZS = u32x2{pk2(z[0], z[1]), pk2(z[2], z[3])};
        asm volatile("" : "+v"(ZS));
    };
    auto stores = [&](int t) {
        const int vz_ = vz, vra0_ = vra0, vra1_ = vra1, vy0_ = vy0, vy1_ = vy1; const unsigned sent_ = SENTV;
        VST8(ZS, vz_, t * (16 * 14 * 512));
        if (g == 0) { VST8(YA0, vy0_, t * (16 * 5 * D * 16)); VST8(YA1, vy1_, t * (16 * 5 * D * 16)); }
        if (w == 1) { const int sp = ((t - 1) & 3) * SLOT; VST4(sent_, vra0_, sp); VST4(sent_, vra1_, sp); }
    };
    int cap = SPIN_CAP;
    const unsigned lsum_s = (unsigned)(size_t)(L + L_S) + l * 4, lsum_o = (unsigned)(size_t)(L + L_O) + (l & 31) * 4;
    const unsigned ldm0a = (unsigned)(size_t)ldm0, ldm1a = (unsigned)(size_t)ldm1;
    auto step = [&](int s, auto kindc) {
        constexpr bool LAST = decltype(kindc)::value == 2, FIRST = decltype(kindc)::value == 0;
        const int so = (s & 3) * SLOT, sU = s * (16 * 5 * D * 16);
        if (!FIRST) wait_until(tref + tauM);
        STAMP(0, tref);
        u32x4 m0, m1;
        VLD16(m0, vm0, so + mc0); VLD16(m1, vm1, so + mc0);
        VWAIT();
        f32x4 te2, to2, te3, to3; u32x4 B0;
        unsigned chk;
#define W_HEAD() asm volatile( \
            "v_mfma_f32_16x16x32_bf16 v[208:211], %[a00], %[m0], 0\n\tv_mfma_f32_16x16x32_bf16 v[212:215], %[a01], %[m0], 0\n\t" \
            "ds_write_b128 %[l0], %[m0]\n\t" \
            "v_mfma_f32_16x16x32_bf16 v[208:211], %[a02], %[m1], v[208:211]\n\t" \
            "v_max3_u32 %[ck], %[x0], %[x1], %[x2]\n\t" \
            "v_mfma_f32_16x16x32_bf16 v[212:215], %[a03], %[m1], v[212:215]\n\t" \
            "ds_write_b128 %[l1], %[m1]\n\t" \
            "v_mfma_f32_16x16x32_bf16 v[216:219], %[a10], %[m0], 0\n\t" \
            "v_max3_u32 %[ck], %[ck], %[x3], %[x4]\n\t" \
            "v_mfma_f32_16x16x32_bf16 v[220:223], %[a11], %[m0], 0\n\t" \
            "v_max3_u32 %[ck], %[ck], %[x5], %[x6]\n\t" \
            "v_mfma_f32_16x16x32_bf16 v[216:219], %[a12], %[m1], v[216:219]\n\t" \
            "v_max_u32 %[ck], %[ck], %[x7]\n\t" \
            "v_mfma_f32_16x16x32_bf16 v[220:223], %[a13], %[m1], v[220:223]\n\t" \
            "v_mfma_f32_16x16x32_bf16 %[e2], %[a20], %[m0], 0\n\t" \
            "v_cndmask_b32 v224, v208, v212, %[pm]\n\tv_cndmask_b32 v225, v209, v213, %[pm]\n\t" \
            "v_mfma_f32_16x16x32_bf16 %[o2], %[a21], %[m0], 0\n\t" \
            "v_cvt_pk_bf16_f32 %[b0], v224, v225\n\tv_cndmask_b32 v224, v210, v214, %[pm]\n\t" \
            "v_mfma_f32_16x16x32_bf16 %[e2], %[a22], %[m1], %[e2]\n\t" \
            "v_cndmask_b32 v225, v211, v215, %[pm]\n\tv_cvt_pk_bf16_f32 %[b1], v224, v225\n\t" \
            "v_mfma_f32_16x16x32_bf16 %[o2], %[a23], %[m1], %[o2]\n\t" \
            "v_mfma_f32_16x16x32_bf16 %[e3], %[a30], %[m0], 0\n\t" \
            "v_cndmask_b32 v224, v216, v220, %[pm]\n\tv_cndmask_b32 v225, v217, v221, %[pm]\n\t" \
            "v_mfma_f32_16x16x32_bf16 %[o3], %[a31], %[m0], 0\n\t" \
            "v_cvt_pk_bf16_f32 %[b2], v224, v225\n\tv_cndmask_b32 v224, v218, v222, %[pm]\n\t" \
            "v_mfma_f32_16x16x32_bf16 %[e3], %[a32], %[m1], %[e3]\n\t" \
            "v_cndmask_b32 v225, v219, v223, %[pm]\n\tv_cvt_pk_bf16_f32 %[b3], v224, v225\n\t" \
            "v_mfma_f32_16x16x32_bf16 %[o3], %[a33], %[m1], %[o3]" \
            : [e2] "=&v"(te2), [o2] "=&v"(to2), [e3] "=&v"(te3), [o3] "=&v"(to3), [b0] "=&v"(B0.x), [b1] "=&v"(B0.y), [b2] "=&v"(B0.z), [b3] "=&v"(B0.w), [ck] "=&v"(chk) \
            : [a00] "a"(AT[0][0]), [a01] "a"(AT[0][1]), [a02] "a"(AT[0][2]), [a03] "a"(AT[0][3]), [a10] "a"(AT[1][0]), [a11] "a"(AT[1][1]), [a12] "a"(AT[1][2]), [a13] "a"(AT[1][3]), \
              [a20] "a"(AT[2][0]), [a21] "a"(AT[2][1]), [a22] "a"(AT[2][2]), [a23] "a"(AT[2][3]), [a30] "a"(AT[3][0]), [a31] "a"(AT[3][1]), [a32] "a"(AT[3][2]), [a33] "a"(AT[3][3]), \
              [m0] "v"(m0), [m1] "v"(m1), [pm] "s"(parm), [l0] "v"(ldm0a), [l1] "v"(ldm1a), \
              [x0] "v"(m0.x), [x1] "v"(m0.y), [x2] "v"(m0.z), [x3] "v"(m0.w), [x4] "v"(m1.x), [x5] "v"(m1.y), [x6] "v"(m1.z), [x7] "v"(m1.w) \
            : "memory", "v208", "v209", "v210", "v211", "v212", "v213", "v214", "v215", "v216", "v217", "v218", "v219", "v220", "v221", "v222", "v223", "v224", "v225")
        W_HEAD();
        COUNT(8, 1);
        if (__builtin_expect(__any(chk == SENT), 0)) {
            int att = 0;
#pragma unroll 1
            do {
                COUNT(8, 1);
                asm volatile("buffer_load_dwordx4 %0, %2, %4, %5 offen sc1\n\tbuffer_load_dwordx4 %1, %3, %4, %5 offen sc1\n\ts_waitcnt vmcnt(0)" : "=&v"(m0), "=&v"(m1) : "v"(vm0), "v"(vm1), "s"(rs), "s"(so + MCOPY - mc0) : "memory");
            } while (bad2(m0, m1) && ++att < cap);
            if (att >= cap) { note_timeout(rs, 0x100 + s); cap = 0; }
            W_HEAD();
        }
        STAMP(1, ta);
        lds_barrier();
        unsigned t2 = 0;
        if (!LAST) {
            asm volatile(
                "ds_read_b128 v[232:235], %[a0]\n\tds_read_b128 v[236:239], %[a1]\n\t"
                "v_cndmask_b32 v224, %[e20], %[o20], %[pm]\n\tv_cndmask_b32 v225, %[e21], %[o21], %[pm]\n\tv_cvt_pk_bf16_f32 v228, v224, v225\n\t"
                "v_cndmask_b32 v224, %[e22], %[o22], %[pm]\n\tv_cndmask_b32 v225, %[e23], %[o23], %[pm]\n\tv_cvt_pk_bf16_f32 v229, v224, v225\n\t"
                "v_cndmask_b32 v224, %[e30], %[o30], %[pm]\n\tv_cndmask_b32 v225, %[e31], %[o31], %[pm]\n\tv_cvt_pk_bf16_f32 v230, v224, v225\n\t"
                "v_cndmask_b32 v224, %[e32], %[o32], %[pm]\n\tv_cndmask_b32 v225, %[e33], %[o33], %[pm]\n\tv_cvt_pk_bf16_f32 v231, v224, v225\n\t"
                "s_waitcnt lgkmcnt(0)\n\t"
                "v_mfma_f32_16x16x32_bf16 v[240:243], v[232:235], %[b0], 0\n\t"
                "v_mfma_f32_16x16x32_bf16 v[240:243], v[236:239], v[228:231], v[240:243]\n\t"
                "s_nop 7\n\t"
                "v_add_f32_dpp v240, v240, v240 row_ror:8 row_mask:0xf bank_mask:0xf\n\tv_add_f32_dpp v241, v241, v241 row_ror:8 row_mask:0xf bank_mask:0xf\n\t"
                "v_add_f32_dpp v242, v242, v242 row_ror:8 row_mask:0xf bank_mask:0xf\n\tv_add_f32_dpp v243, v243, v243 row_ror:8 row_mask:0xf bank_mask:0xf\n\t"
                "v_permlane32_swap_b32 v240, v241\n\ts_nop 0\n\tv_permlane32_swap_b32 v242, v243\n\t"
                "v_add_f32 v240, v240, v241\n\tv_add_f32 v242, v242, v243\n\tv_cndmask_b32 v240, v240, v242, %[pm]\n\t"
                "ds_write_b32 %[dst], v240\n\t"
                "s_waitcnt lgkmcnt(0)\n\ts_barrier"
                :: [a0] "v"(a0a), [a1] "v"(a1a), [b0] "v"(B0), [pm] "s"(parm), [dst] "v"(lds_sa),
                   [e20] "v"(te2[0]), [e21] "v"(te2[1]), [e22] "v"(te2[2]), [e23] "v"(te2[3]), [o20] "v"(to2[0]), [o21] "v"(to2[1]), [o22] "v"(to2[2]), [o23] "v"(to2[3]),
                   [e30] "v"(te3[0]), [e31] "v"(te3[1]), [e32] "v"(te3[2]), [e33] "v"(te3[3]), [o30] "v"(to3[0]), [o31] "v"(to3[1]), [o32] "v"(to3[2]), [o33] "v"(to3[3])
                : "memory", "v224", "v225", "v228", "v229", "v230", "v231", "v232", "v233", "v234", "v235", "v236", "v237", "v238", "v239", "v240", "v241", "v242", "v243");
#define PUBLISH(lsum, o1, o2, o3, voff, soff) asm volatile( \
                "ds_read_b32 v224, %[a]\n\tds_read_b32 v225, %[a] offset:" #o1 "\n\tds_read_b32 v226, %[a] offset:" #o2 "\n\tds_read_b32 v227, %[a] offset:" #o3 "\n\t" \
                "s_waitcnt lgkmcnt(2)\n\tv_add_f32 v224, v224, v225\n\ts_waitcnt lgkmcnt(0)\n\tv_add_f32 v226, v226, v227\n\tv_add_f32 v224, v224, v226\n\ts_nop 1\n\t" \
                "v_mov_b32_dpp v225, v224 quad_perm:[0,0,2,2] row_mask:0xf bank_mask:0xf\n\tv_mov_b32_dpp v226, v224 quad_perm:[1,1,3,3] row_mask:0xf bank_mask:0xf\n\t" \
                "v_cvt_pk_bf16_f32 v224, v225, v226\n\tv_min_u32 v224, -2, v224\n\tbuffer_store_dword v224, %[v], %[r], %[s] offen sc1" \
                :: [a] "v"(lsum), [v] "v"(voff), [r] "s"(rs), [s] "s"(soff) : "memory", "v224", "v225", "v226", "v227")
            if (w == 0) PUBLISH(lsum_s, 256, 512, 768, vspub, so);
            t2 = ct() - (w == 0 ? 150 : 0);
            STAMP(2, ta);
        }
        SCHED();
        u32x4 U0, U1; unsigned flg;
        if (FIRST) {
            int att = 0; unsigned f2;
#pragma unroll 1
            do {
                asm volatile("buffer_load_dword %0, %1, %2, 0 offen sc1\n\ts_waitcnt vmcnt(0)" : "=&v"(f2) : "v"(vflag), "s"(rs) : "memory");
            } while (__any(f2 == 0) && ++att < cap);
            if (att >= cap) { note_timeout(rs, 0x200); cap = 0; }
        }
        if (!LAST) { VLD16(U0, vu0, sU); VLD16(U1, vu1, sU); VLD4(flg, vflag, (s + 1) * 1024); }
        if (FIRST) {
#pragma unroll
            for (int ks = 0; ks < 14; ++ks) AZ[ks] = gld16(p.ws + OFF_A + piece(h, 2 * c + (w >> 1), 4 * ks + q, 16 * (w & 1) + r16));
            SCHED();
        }
        f32x4 ye0, yo0, ye1, yo1;
#pragma unroll
        for (int i = 0; i < 7; ++i) mf[i] = *(const LAS u32x4*)(L + L_M + i * 1024 + l * 16);
#define Y_STEP0(i) asm volatile("v_mfma_f32_16x16x32_bf16 %0, %4, %5, 0\n\tv_mfma_f32_16x16x32_bf16 %1, %4, %6, 0\n\tv_mfma_f32_16x16x32_bf16 %2, %4, %7, 0\n\tv_mfma_f32_16x16x32_bf16 %3, %4, %8, 0" \
            : "=&v"(ye0), "=&v"(yo0), "=&v"(ye1), "=&v"(yo1) : "v"(mf[i]), "a"(BT[0][2 * i]), "a"(BT[0][2 * i + 1]), "a"(BT[1][2 * i]), "a"(BT[1][2 * i + 1]))
#define Y_STEP(i) asm volatile("v_mfma_f32_16x16x32_bf16 %0, %4, %5, %0\n\tv_mfma_f32_16x16x32_bf16 %1, %4, %6, %1\n\tv_mfma_f32_16x16x32_bf16 %2, %4, %7, %2\n\tv_mfma_f32_16x16x32_bf16 %3, %4, %8, %3" \
            : "+v"(ye0), "+v"(yo0), "+v"(ye1), "+v"(yo1) : "v"(mf[i]), "a"(BT[0][2 * i]), "a"(BT[0][2 * i + 1]), "a"(BT[1][2 * i]), "a"(BT[1][2 * i + 1]))
        Y_STEP0(0); Y_STEP(1); Y_STEP(2); Y_STEP(3); Y_STEP(4);
        SCHED();
        u32x4 s0, s1, s2;
        if (!LAST) {
            STAMP(3, ta);
            wait_until(t2 + tauS);
            STAMP(4, ta);
            VLD16(s0, vs0, so + sc0); VLD16(s1, vs1, so + sc0); VLD16(s2, vs2, so + sc0);
        }
        SCHED();
        Y_STEP(5); Y_STEP(6);
        asm volatile("s_nop 7" : "+v"(ye0), "+v"(yo0), "+v"(ye1), "+v"(yo1));
        {
            float y[2][4];
#pragma unroll
            for (int r = 0; r < 4; ++r) {
                const auto x = __builtin_amdgcn_permlane32_swap(__float_as_uint(yo0[r]), __float_as_uint(yo0[r]), false, false); y[0][r] = ye0[r] + __uint_as_float(x[1]);
                const auto z = __builtin_amdgcn_permlane32_swap(__float_as_uint(yo1[r]), __float_as_uint(yo1[r]), false, false); y[1][r] = ye1[r] + __uint_as_float(z[1]);
            }
            YA0 = u32x2{pk2(y[0][0], y[0][1]), pk2(y[0][2], y[0][3])}; YA1 = u32x2{pk2(y[1][0], y[1][1]), pk2(y[1][2], y[1][3])};
            asm volatile("" : "+v"(YA0), "+v"(YA1));
        }
        if (LAST) return;
        u32x2 YM0 = l < 32 ? YA0 : u32x2{0, 0}, YM1 = l < 32 ? YA1 : u32x2{0, 0};
        int vmp = toring ? vmring + ((s + 1) & 3) * SLOT : vmglob + (s + 1) * 16384;
        asm volatile("" : "+v"(YM0), "+v"(YM1), "+v"(vmp));
        SCHED();
        STAMP(5, ta);
        VWAIT();
        f32x4 dxa, dxb, ds;
#define B_HEAD() asm volatile( \
            "v_mfma_f32_16x16x32_bf16 %0, %4, %6, %10\n\tv_mfma_f32_16x16x32_bf16 %0, %5, %7, %0\n\t" \
            "v_max3_u32 %3, %12, %13, %14\n\tv_max3_u32 %3, %3, %15, %16\n\t" \
            "v_mfma_f32_16x16x32_bf16 %1, %8, %11, 0\n\t" \
            "v_max3_u32 %3, %3, %17, %18\n\tv_max3_u32 %3, %3, %19, %20\n\t" \
            "v_mfma_f32_16x16x32_bf16 %2, %9, %11, 0\n\t" \
            "v_max3_u32 %3, %3, %21, %22\n\tv_max_u32 %3, %3, %23" \
            : "=&v"(ds), "=&v"(dxa), "=&v"(dxb), "=&v"(chk) \
            : "v"(SEL0), "v"(SEL1), "v"(s0), "v"(s1), "v"(U0), "v"(U1), "v"(CINIT), "v"(s2), \
              "v"(s0.x), "v"(s0.y), "v"(s0.z), "v"(s0.w), "v"(s1.x), "v"(s1.y), "v"(s1.z), "v"(s1.w), "v"(s2.x), "v"(s2.y), "v"(s2.z), "v"(s2.w))
        B_HEAD();
        COUNT(9, 1);
        if (__builtin_expect(__any(chk == SENT), 0)) {
            int att = 0;
#pragma unroll 1
            do {
                COUNT(9, 1);
                asm volatile("buffer_load_dwordx4 %0, %3, %6, %7 offen sc1\n\tbuffer_load_dwordx4 %1, %4, %6, %7 offen sc1\n\tbuffer_load_dwordx4 %2, %5, %6, %7 offen sc1\n\ts_waitcnt vmcnt(0)"
                             : "=&v"(s0), "=&v"(s1), "=&v"(s2) : "v"(vs0), "v"(vs1), "v"(vs2), "s"(rs), "s"(so + SCOPY - sc0) : "memory");
            } while (__any(max(max(mx4(s0), mx4(s1)), mx4(s2)) == SENT) && ++att < cap);
            if (att >= cap) { note_timeout(rs, 0x300 + s); cap = 0; }
            B_HEAD();
        }
        STAMP(6, ta);
        asm volatile(
            "v_max3_f32 v224, %[d0], %[d1], %[d2]\n\t"
            "v_max_f32 v224, v224, %[d3]\n\t"
            "v_mov_b32 v225, v224\n\t"
            "s_nop 1\n\t"
            "v_permlane32_swap_b32 v224, v225\n\t"
            "v_max_f32 v224, v224, v225\n\t"
            "v_mov_b32 v225, v224\n\t"
            "s_nop 1\n\t"
            "v_permlane16_swap_b32 v224, v225\n\t"
            "v_max_f32 v224, v224, v225\n\t"
            "v_mul_f32 v224, 0xbdb8aa3b, v224\n\t"
            "v_fmamk_f32 v226, %[d0], 0x3db8aa3b, v224\n\t"
            "v_fmamk_f32 v227, %[d1], 0x3db8aa3b, v224\n\t"
            "v_exp_f32 v226, v226\n\t"
            "v_fmamk_f32 v228, %[d2], 0x3db8aa3b, v224\n\t"
            "v_exp_f32 v227, v227\n\t"
            "v_fmamk_f32 v229, %[d3], 0x3db8aa3b, v224\n\t"
            "v_exp_f32 v228, v228\n\t"
            "v_exp_f32 v229, v229\n\t"
            "v_cvt_pk_bf16_f32 v230, v226, v227\n\t"
            "v_cvt_pk_bf16_f32 v231, v228, v229\n\t"
            "v_cndmask_b32 v224, %[xa0], %[xb0], %[hb]\n\tv_cndmask_b32 v225, %[xa1], %[xb1], %[hb]\n\t"
            "v_mfma_f32_16x16x16_bf16 v[232:235], %[one], v[230:231], 0\n\t"
            "v_mfma_f32_16x16x16_bf16 v[236:239], %[aw], v[230:231], 0\n\t"
            "v_mfma_f32_16x16x16_bf16 v[240:243], %[y0], v[230:231], 0\n\t"
            "v_mfma_f32_16x16x16_bf16 v[244:247], %[y1], v[230:231], 0\n\t"
            "v_cndmask_b32 v226, %[xa2], %[xb2], %[hb]\n\tv_cndmask_b32 v227, %[xa3], %[xb3], %[hb]\n\t"
            "s_nop 2\n\t"
            "v_rcp_f32 v228, v232\n\t"
            "s_nop 0\n\t"
            "v_cndmask_b32 v228, 0, v228, %[hv]\n\t"
            "v_mul_f32 v229, v236, v228\n\t"
            "v_cndmask_b32 v240, v240, v244, %[hb]\n\tv_cndmask_b32 v241, v241, v245, %[hb]\n\t"
            "v_cndmask_b32 v242, v242, v246, %[hb]\n\tv_cndmask_b32 v243, v243, v247, %[hb]\n\t"
            "v_pk_mul_f32 v[240:241], v[240:241], v[228:229] op_sel_hi:[1,0]\n\tv_pk_mul_f32 v[242:243], v[242:243], v[228:229] op_sel_hi:[1,0]\n\t"
            "v_pk_fma_f32 v[240:241], v[224:225], v[228:229], v[240:241] op_sel:[0,1,0] op_sel_hi:[1,1,1]\n\t"
            "v_pk_fma_f32 v[242:243], v[226:227], v[228:229], v[242:243] op_sel:[0,1,0] op_sel_hi:[1,1,1]\n\t"
            "s_nop 0\n\t"
            "v_add_f32_dpp v240, v240, v240 row_ror:4 row_mask:0xf bank_mask:0xf\n\tv_add_f32_dpp v241, v241, v241 row_ror:4 row_mask:0xf bank_mask:0xf\n\t"
            "v_add_f32_dpp v242, v242, v242 row_ror:4 row_mask:0xf bank_mask:0xf\n\tv_add_f32_dpp v243, v243, v243 row_ror:4 row_mask:0xf bank_mask:0xf\n\t"
            "v_add_f32_dpp v240, v240, v240 row_ror:8 row_mask:0xf bank_mask:0xf\n\tv_add_f32_dpp v241, v241, v241 row_ror:8 row_mask:0xf bank_mask:0xf\n\t"
            "v_add_f32_dpp v242, v242, v242 row_ror:8 row_mask:0xf bank_mask:0xf\n\tv_add_f32_dpp v243, v243, v243 row_ror:8 row_mask:0xf bank_mask:0xf\n\t"
            "v_permlane32_swap_b32 v240, v241\n\ts_nop 0\n\tv_permlane32_swap_b32 v242, v243\n\t"
            "v_add_f32 v240, v240, v241\n\tv_add_f32 v242, v242, v243\n\tv_cndmask_b32 v240, v240, v242, %[h1]\n\t"
            "ds_write_b32 %[dst], v240\n\t"
            "s_waitcnt lgkmcnt(0)\n\ts_barrier"
            :: [d0] "v"(ds[0]), [d1] "v"(ds[1]), [d2] "v"(ds[2]), [d3] "v"(ds[3]), [one] "v"(AONE), [aw] "v"(AW), [y0] "v"(YM0), [y1] "v"(YM1),
               [hb] "s"(hbm), [hv] "s"(hvm), [h1] "s"(h1m), [dst] "v"(lds_oa),
               [xa0] "v"(dxa[0]), [xa1] "v"(dxa[1]), [xa2] "v"(dxa[2]), [xa3] "v"(dxa[3]), [xb0] "v"(dxb[0]), [xb1] "v"(dxb[1]), [xb2] "v"(dxb[2]), [xb3] "v"(dxb[3])
            : "memory", "v224", "v225", "v226", "v227", "v228", "v229", "v230", "v231", "v232", "v233", "v234", "v235", "v236", "v237", "v238", "v239",
              "v240", "v241", "v242", "v243", "v244", "v245", "v246", "v247");
        if (w == 0) PUBLISH(lsum_o, 128, 256, 384, vmp, 0);
        tref = ct() - (w == 0 ? 150 : 0);
        STAMP(7, ta);
        SCHED();
        tail();
        SCHED();
        stores(s);
        if (__builtin_expect(__any(flg == 0), 0)) {
            int att = 0; unsigned f2;
#pragma unroll 1
            do {
                asm volatile("buffer_load_dword %0, %1, %2, %3 offen sc1\n\ts_waitcnt vmcnt(0)" : "=&v"(f2) : "v"(vflag), "s"(rs), "s"((s + 1) * 1024) : "memory");
                __builtin_amdgcn_s_sleep(1);
            } while (__any(f2 == 0) && ++att < cap);
            if (att >= cap) { note_timeout(rs, 0x200 + s); cap = 0; }
        }
    };
    step(0, IntC<0>{});
#pragma unroll
    for (int ks = 0; ks < 14; ++ks) asm volatile("" : "+a"(AZ[ks]));
#pragma unroll 1
    for (int s = 1; s < NSEG - 1; ++s) step(s, IntC<1>{});
    step(NSEG - 1, IntC<2>{});
    tail();
    stores(NSEG - 1);
    if (w == 0) { const unsigned z = 0; VST4(z, toring ? OOB : vmglob, NSEG * 16384); }
    DRAIN();
    lds_barrier();
    if (w == 0) { const unsigned z = 0; VST4(z, toring ? OOB : vmglob, (NSEG + 1) * 16384); }
    DRAIN();
}

DI void chain_mx(const Params& p, int h, LAS unsigned char* L) {
    const int tid = threadIdx.x, l = tid & 63, w = __builtin_amdgcn_readfirstlane(tid >> 6), q = l >> 4;
    const int parl = (l >> 3) & 1;
    const rsrc_t rs = __builtin_amdgcn_make_buffer_rsrc(p.ws, 0, (int)WS_END, 0x00020000);
    const int ring = (int)OFF_RING + wait_parity(rs) * 8192;
    const int lo_m = (4 * parl + q) * 128 + (l & 7) * 16;
    const int vm0 = ring + min(2 * w, 6) * 1024 + lo_m, vm1 = w < 3 ? ring + (2 * w + 1) * 1024 + lo_m : OOB;
    int vz[2][4];
#pragma unroll
    for (int kt = 0; kt < 2; ++kt)
#pragma unroll
        for (int kk = 0; kk < 4; ++kk) vz[kt][kk] = (int)OFF_Z + h * 28 * 1024 + l * 16 + (kt * 14 + min(4 * w + kk, 13)) * 1024;
    const int vflag = (int)OFF_FLAG_ZU + (h * 7 + min(l & 7, 6)) * 4;
    const int tok = 2 * w + (l >> 5), key = l & 31;
    const int vppub = ring + SOFF + (l & 1) * SCOPY + s_piece(h, 8 + (key >> 3), tok) + ((key & 7) >> 1) * 4;
    const int vlpub = key < 8 ? ring + SOFF + ((key >> 2) & 1) * SCOPY + s_piece(h, 7, tok) + (key & 3) * 4 : OOB;
    const unsigned long long parm = __builtin_amdgcn_ballot_w64(parl != 0), k0m = __builtin_amdgcn_ballot_w64((key & 3) == 0), k1m = __builtin_amdgcn_ballot_w64((key & 3) == 1);
    LAS unsigned char* const lds_w = L + L_X + w * 1024 + ((l & 7) * 32 + 16 * parl + 4 * q) * 4;
    const LAS float* const xp = (const LAS float*)(L + L_X) + tok * 32 + key;
    const unsigned SENTV = SENT;
    int tau = 0;
    unsigned slot = 0;
#define XSTAMP(i)
    int natt = 0;
    unsigned xs_z = 0, xs_f = 0;
    u32x4 Z00, Z01, Z02, Z03, Z10, Z11, Z12, Z13;
#define LOAD_Z(sz) do { VLD16(Z00, vz[0][0], sz); VLD16(Z01, vz[0][1], sz); VLD16(Z02, vz[0][2], sz); VLD16(Z03, vz[0][3], sz); \
                        VLD16(Z10, vz[1][0], sz); VLD16(Z11, vz[1][1], sz); VLD16(Z12, vz[1][2], sz); VLD16(Z13, vz[1][3], sz); } while (0)
#define WAIT_FLAG(sg) _Pragma("unroll 1") for (int att = 0;; ++att) { unsigned f2; asm volatile("buffer_load_dword %0, %1, %2, %3 offen sc1\n\ts_waitcnt vmcnt(0)" : "=&v"(f2) : "v"(vflag), "s"(rs), "s"((sg) * 1024) : "memory"); \
        if (!__any(f2 == 0)) break; if (att > SPIN_CAP) { note_timeout(rs, 0x400 + (sg)); return; } __builtin_amdgcn_s_sleep(2); }
    unsigned flg;
    WAIT_FLAG(0);
    LOAD_Z(0);
    VLD4(flg, vflag, 1024);
    tau = p.tauXm;
    unsigned tref = ct();
    for (int s = 0; s < NSEG - 1; ++s) {
        const int so = (s & 3) * SLOT;
        if (s > 0) wait_until(tref + tau);
        XSTAMP(0);
        u32x4 m0, m1;
        VLD16(m0, vm0, so); VLD16(m1, vm1, so);
        VWAIT();
        f32x4 te0, to0, te1, to1; unsigned chk;
#define X_HEAD() asm volatile( \
            "v_mfma_f32_16x16x32_bf16 %0, %5, %13, 0\n\tv_mfma_f32_16x16x32_bf16 %1, %6, %13, 0\n\t" \
            "v_max3_u32 %4, %15, %16, %17\n\t" \
            "v_mfma_f32_16x16x32_bf16 %2, %9, %13, 0\n\t" \
            "v_max3_u32 %4, %4, %18, %19\n\t" \
            "v_mfma_f32_16x16x32_bf16 %3, %10, %13, 0\n\t" \
            "v_max3_u32 %4, %4, %20, %21\n\t" \
            "v_mfma_f32_16x16x32_bf16 %0, %7, %14, %0\n\t" \
            "v_max_u32 %4, %4, %22\n\t" \
            "v_mfma_f32_16x16x32_bf16 %1, %8, %14, %1\n\tv_mfma_f32_16x16x32_bf16 %2, %11, %14, %2\n\tv_mfma_f32_16x16x32_bf16 %3, %12, %14, %3" \
            : "=&v"(te0), "=&v"(to0), "=&v"(te1), "=&v"(to1), "=&v"(chk) \
            : "v"(Z00), "v"(Z01), "v"(Z02), "v"(Z03), "v"(Z10), "v"(Z11), "v"(Z12), "v"(Z13), "v"(m0), "v"(m1), \
              "v"(m0.x), "v"(m0.y), "v"(m0.z), "v"(m0.w), "v"(m1.x), "v"(m1.y), "v"(m1.z), "v"(m1.w))
        X_HEAD();
        int att = 0;
        if (__any(chk == SENT)) {
#pragma unroll 1
            for (att = 1;; ++att) {
                asm volatile("buffer_load_dwordx4 %0, %2, %4, %5 offen sc1\n\tbuffer_load_dwordx4 %1, %3, %4, %5 offen sc1\n\ts_waitcnt vmcnt(0)" : "=&v"(m0), "=&v"(m1) : "v"(vm0), "v"(vm1), "s"(rs), "s"(so + MCOPY) : "memory");
                if (!bad2(m0, m1)) break;
                if (att > SPIN_CAP) { note_timeout(rs, 0x500 + s); return; }
            }
            X_HEAD();
            asm volatile("s_nop 1");
        }
        XSTAMP(1);
        f32x4 v;
        {
            float a0, a1, a2, a3, b0, b1, b2, b3;
            asm volatile(
                "s_nop 5\n\t"
                "v_cndmask_b32 %4, %[e10], %[o00], %[pm]\n\tv_cndmask_b32 %5, %[e11], %[o01], %[pm]\n\tv_cndmask_b32 %6, %[e12], %[o02], %[pm]\n\tv_cndmask_b32 %7, %[e13], %[o03], %[pm]\n\t"
                "v_cndmask_b32 %0, %[e00], %[o10], %[pm]\n\tv_cndmask_b32 %1, %[e01], %[o11], %[pm]\n\tv_cndmask_b32 %2, %[e02], %[o12], %[pm]\n\tv_cndmask_b32 %3, %[e03], %[o13], %[pm]\n\t"
                "v_add_f32_dpp %0, %4, %0 row_ror:8 row_mask:0xf bank_mask:0xf\n\tv_add_f32_dpp %1, %5, %1 row_ror:8 row_mask:0xf bank_mask:0xf\n\t"
                "v_add_f32_dpp %2, %6, %2 row_ror:8 row_mask:0xf bank_mask:0xf\n\tv_add_f32_dpp %3, %7, %3 row_ror:8 row_mask:0xf bank_mask:0xf"
                : "=&v"(a0), "=&v"(a1), "=&v"(a2), "=&v"(a3), "=&v"(b0), "=&v"(b1), "=&v"(b2), "=&v"(b3)
                : [pm] "s"(parm),
                  [e00] "v"(te0[0]), [e01] "v"(te0[1]), [e02] "v"(te0[2]), [e03] "v"(te0[3]), [o00] "v"(to0[0]), [o01] "v"(to0[1]), [o02] "v"(to0[2]), [o03] "v"(to0[3]),
                  [e10] "v"(te1[0]), [e11] "v"(te1[1]), [e12] "v"(te1[2]), [e13] "v"(te1[3]), [o10] "v"(to1[0]), [o11] "v"(to1[1]), [o12] "v"(to1[2]), [o13] "v"(to1[3]));
            v = f32x4{a0, a1, a2, a3};
        }
        *(LAS f32x4*)lds_w = v;
        lds_barrier();
        {
            const float sv = ((xp[0] + xp[256]) + (xp[512] + xp[768])) * SC2;
            float mx, t, e, sum, pn, lg;
            asm volatile(
                "s_nop 1\n\t"
                "v_max_f32_dpp %0, %4, %4 row_ror:1 row_mask:0xf bank_mask:0xf\n\ts_nop 1\n\t"
                "v_max_f32_dpp %0, %0, %0 row_ror:2 row_mask:0xf bank_mask:0xf\n\ts_nop 1\n\t"
                "v_max_f32_dpp %0, %0, %0 row_ror:4 row_mask:0xf bank_mask:0xf\n\ts_nop 1\n\t"
                "v_max_f32_dpp %0, %0, %0 row_ror:8 row_mask:0xf bank_mask:0xf\n\t"
                "v_mov_b32 %1, %0\n\ts_nop 1\n\t"
                "v_permlane16_swap_b32 %0, %1\n\t"
                "v_max_f32 %0, %0, %1\n\t"
                "v_sub_f32 %2, %4, %0\n\t"
                "v_exp_f32 %2, %2\n\ts_nop 1\n\t"
                "v_add_f32_dpp %3, %2, %2 row_ror:1 row_mask:0xf bank_mask:0xf\n\ts_nop 1\n\t"
                "v_add_f32_dpp %3, %3, %3 row_ror:2 row_mask:0xf bank_mask:0xf\n\ts_nop 1\n\t"
                "v_add_f32_dpp %3, %3, %3 row_ror:4 row_mask:0xf bank_mask:0xf\n\ts_nop 1\n\t"
                "v_add_f32_dpp %3, %3, %3 row_ror:8 row_mask:0xf bank_mask:0xf\n\t"
                "v_mov_b32 %1, %3\n\ts_nop 1\n\t"
                "v_permlane16_swap_b32 %3, %1\n\t"
                "v_add_f32 %3, %3, %1"
                : "=&v"(mx), "=&v"(t), "=&v"(e), "=&v"(sum) : "v"(sv));
            pn = e * __builtin_amdgcn_rcpf(sum);
            const unsigned pv = min(pk2(dpp<QP0022>(pn), dpp<QP1133>(pn)), SENT - 1);
            VST4(pv, vppub, so);
            lg = __builtin_amdgcn_logf(sum);
            const float lse = (mx + lg) * (1.0f / SC2);
            const float hi = __uint_as_float(pk2(lse, 0.f) << 16), r1 = lse - hi, mid = __uint_as_float(pk2(r1, 0.f) << 16), lo = r1 - mid;
            const unsigned d0 = pk2(hi, mid), d1 = pk2(lo, 0.f);
            const unsigned lv = min((key & 3) == 0 ? d0 : ((key & 3) == 1 ? d1 : 0u), SENT - 1);
            VST4(lv, vlpub, so);
        }
        XSTAMP(2);
        tref = ct();
        if (s > 2) tau = min(max(tau + (att == 0 ? -XDN : XUP), p.tauXm - 400), p.tauXm + 400);
        if (s >= 8) natt += att + 1;
        lds_barrier();
        __builtin_amdgcn_s_sleep(2);
        { const int sp = ((s - 1) & 3) * SLOT; VST4(SENTV, vppub, sp); VST4(SENTV, vlpub, sp); }
        if (__any(flg == 0)) { WAIT_FLAG(s + 1); }
        LOAD_Z((s + 1) * (16 * 28 * 1024));
        VLD4(flg, vflag, min(s + 2, NSEG - 1) * 1024);
    }
    DRAIN();
}

constexpr int XB = 32768;
DI void x_dma(const Params& p, int s, LAS unsigned char* buf, int w, int l) {
#pragma unroll
    for (int i = 0; i < 8; ++i) {
        const int r = 8 * w + i;
        int ch = l ^ (r & 15);
        ch = ch < 56 ? ch : ch - 8;
        __builtin_amdgcn_global_load_lds((const GAS unsigned*)(p.x + ((size_t)s * 32 + r) * D + ch * 8), (LAS unsigned*)(buf + r * 1024), 16, 0, 0);
    }
}
DI u32x4 swz_lo(u32x2& a, u32x2& b) {
    const auto x = __builtin_amdgcn_permlane32_swap(a.x, b.x, false, false);
    const auto y = __builtin_amdgcn_permlane32_swap(a.y, b.y, false, false);
    return u32x4{x[0], y[0], x[1], y[1]};
}
template <int NKS> DI void load_tr(u32x4 (&W)[NKS], const u16* src, int ld, LAS unsigned char* stg, int l) {
    constexpr int HALF = NKS / 2;
#pragma unroll
    for (int hf = 0; hf < 2; ++hf) {
        asm volatile("s_waitcnt lgkmcnt(0)" ::: "memory");
        for (int i = 0; i < HALF; ++i) {
            const int r = 16 * i + (l >> 2);
            *(LAS u32x4*)(stg + r * 64 + (l & 3) * 16) = gld16(src + (size_t)(hf * HALF * 16 + r) * ld + (l & 3) * 8);
        }
        asm volatile("s_waitcnt lgkmcnt(0)" ::: "memory");
#pragma unroll
        for (int k = 0; k < HALF; ++k) {
            const LAS unsigned char* a = stg + (16 * k + 8 * (l >> 5) + ((l & 15) >> 2)) * 64 + ((l >> 4) & 1) * 32 + (l & 3) * 8;
            const u32x2 x = __builtin_bit_cast(u32x2, __builtin_amdgcn_ds_read_tr16_b64_v4i16((LAS s16x4*)a));
            const u32x2 y = __builtin_bit_cast(u32x2, __builtin_amdgcn_ds_read_tr16_b64_v4i16((LAS s16x4*)(a + 4 * 64)));
            W[hf * HALF + k] = u32x4{x.x, x.y, y.x, y.y};
        }
    }
}

DI void x_dma1(const Params& p, int s, LAS unsigned char* buf, int r, int ch8) {
    __builtin_amdgcn_global_load_lds((const GAS unsigned*)(p.x + ((size_t)s * 32 + r) * D + ch8), (LAS unsigned*)(buf + r * 1024), 16, 0, 0);
}
template <int KIND>
DI void stream_x(const Params& p, int cu, LAS unsigned char* L) {
    const int tid = threadIdx.x, l = tid & 63, w = __builtin_amdgcn_readfirstlane(tid >> 6), hi = l >> 5, tk = l & 31;
    const rsrc_t rs = __builtin_amdgcn_make_buffer_rsrc(p.ws, 0, (int)WS_END, 0x00020000);
    const int wid = cu * 4 + w;
    u32x4 W[2][28];
    int h = 0, b = 0;
    x_dma(p, 0, L, w, l);
    if (KIND < 2) {
        const int T = wid * 2; h = T / 14; b = T % 14;
        const unsigned char* src = p.ws + (KIND == 0 ? OFF_A : OFF_BT);
#pragma unroll
        for (int ks = 0; ks < 28; ++ks)
#pragma unroll
            for (int t = 0; t < 2; ++t) W[t][ks] = gld16(src + piece(h, b + t, 2 * ks + hi, tk));
        SCHED();
    } else {
        LAS unsigned char* stg = L + 2 * XB + w * (14 * 16 * 64);
        load_tr<28>(W[0], p.Wg + 32 * wid, FF, stg, l);
        load_tr<28>(W[1], p.Wu + 32 * wid, FF, stg, l);
    }
    const int vflag = KIND == 2 ? (int)OFF_FLAG_H + wid * 4 : (int)OFF_FLAG_ZU + ((KIND == 1 ? 112 : 0) + wid) * 4;
    const int fstride = KIND == 2 ? 256 : 1024;
    constexpr int NST = KIND == 2 ? 2 : 4;
    const LAS unsigned char* xrow = L + tk * 1024;
    int xo[8], ch8[8];
#pragma unroll
    for (int k = 0; k < 8; ++k) { xo[k] = ((2 * k + hi) ^ (tk & 15)) * 16; int ch = l ^ ((8 * w + k) & 15); ch8[k] = (ch < 56 ? ch : ch - 8) * 8; }
    const int vst = KIND == 0 ? (int)OFF_Z + ((h * 2 + (tk >> 4)) * 14 + b) * 1024 + (hi * 16 + (tk & 15)) * 16
                  : KIND == 1 ? (int)OFF_U + ((h * 5 + hi) * D + 32 * b + tk) * 16 : (int)OFF_H + ((4 * wid + hi) * 32 + tk) * 16;
    constexpr int sstr = KIND == 0 ? 16 * 28 * 1024 : KIND == 1 ? 16 * 5 * D * 16 : 224 * 512;
    auto epi = [&](const f32x16 (&a)[2], int s, int i) {
        if (KIND == 0) {
            const int t = i >> 1, P = i & 1;
            const u32x4 v = {pk2(a[t][4 * P + 0], a[t][4 * P + 1]), pk2(a[t][4 * P + 2], a[t][4 * P + 3]), pk2(a[t][8 + 4 * P + 0], a[t][8 + 4 * P + 1]), pk2(a[t][8 + 4 * P + 2], a[t][8 + 4 * P + 3])};
            bst16(v, rs, vst + t * 1024 + P * 512, s * sstr);
        } else if (KIND == 1) {
            const int t = i >> 1, P = i & 1;
            u32x2 x = {pk2(a[t][8 * P + 0], a[t][8 * P + 1]), pk2(a[t][8 * P + 2], a[t][8 * P + 3])};
            u32x2 y = {pk2(a[t][8 * P + 4], a[t][8 * P + 5]), pk2(a[t][8 * P + 6], a[t][8 * P + 7])};
            const u32x4 v = swz_lo(x, y);
            bst16(v, rs, vst + t * 512 + P * (2 * D * 16), s * sstr);
        } else {
            float hv[8];
#pragma unroll
            for (int k = 0; k < 8; ++k) { const float gg = a[0][8 * i + k]; hv[k] = gg * __builtin_amdgcn_rcpf(1.f + __builtin_amdgcn_exp2f(-1.4426950408889634f * gg)) * a[1][8 * i + k]; }
            u32x2 x = {pk2(hv[0], hv[1]), pk2(hv[2], hv[3])}, y = {pk2(hv[4], hv[5]), pk2(hv[6], hv[7])};
            const u32x4 v = swz_lo(x, y);
            bst16(v, rs, vst + i * 1024, s * sstr);
        }
    };
    auto tile = [&](int s, f32x16 (&acc)[2], const f32x16 (&prev)[2], bool first = false) {
        if (first && KIND < 2) asm volatile("s_waitcnt vmcnt(56)" ::: "memory");
        else if (s < 4) { DRAIN(); if (s >= 2 && l == 0) bst4(1u, rs, vflag, (s - 2) * fstride); }
        else { asm volatile("s_waitcnt vmcnt(%0)" :: "n"(NST) : "memory"); if (l == 0) { bst4(1u, rs, vflag, (s - 3) * fstride); if (s == 4) bst4(1u, rs, vflag, 2 * fstride); } }
        lds_barrier();
        const LAS unsigned char* xb = xrow + (s & 1) * XB;
        LAS unsigned char* nb = L + ((s + 1) & 1) * XB;
        const int sn = min(s + 1, NSEG - 1);
        u32x4 xf[4];
#pragma unroll
        for (int k = 0; k < 3; ++k) xf[k] = *(const LAS u32x4*)(xb + xo[k]);
#pragma unroll
        for (int ks = 0; ks < 28; ++ks) {
            if (ks + 3 < 28) xf[(ks + 3) & 3] = *(const LAS u32x4*)(xb + xo[(ks + 3) & 7] + ((ks + 3) >> 3) * 256);
            SCHED();
            if (ks == 0) {
                if (KIND == 1) { mma32_wb<true>(acc[0], xf[0], W[0][0]); mma32_wb<true>(acc[1], xf[0], W[1][0]); }
                else { mma32_wa<true>(acc[0], W[0][0], xf[0]); mma32_wa<true>(acc[1], W[1][0], xf[0]); }
            } else {
                if (KIND == 1) { mma32_wb<false>(acc[0], xf[ks & 3], W[0][ks]); mma32_wb<false>(acc[1], xf[ks & 3], W[1][ks]); }
                else { mma32_wa<false>(acc[0], W[0][ks], xf[ks & 3]); mma32_wa<false>(acc[1], W[1][ks], xf[ks & 3]); }
            }
            SCHED();
            if (ks >= 1 && ks < 9) x_dma1(p, sn, nb, 8 * w + ks - 1, ch8[ks - 1]);
            if (ks >= 10 && (ks - 10) % (KIND == 2 ? 8 : 4) == 0 && (ks - 10) / (KIND == 2 ? 8 : 4) < NST && s > 1) epi(prev, s - 1, (ks - 10) / (KIND == 2 ? 8 : 4));
            SCHED();
        }
    };
    f32x16 accA[2], accB[2];
    tile(0, accB, accA, true);
    mma32_done(accB[0], accB[1]);
#pragma unroll
    for (int i = 0; i < NST; ++i) epi(accB, 0, i);
    DRAIN();
    if (l == 0) bst4(1u, rs, vflag, 0);
    tile(1, accB, accA);
#pragma unroll 1
    for (int s = 2; s < NSEG; s += 2) { tile(s, accA, accB); tile(s + 1, accB, accA); }
    mma32_done(accB[0], accB[1]);
#pragma unroll
    for (int i = 0; i < NST; ++i) epi(accB, NSEG - 1, i);
    DRAIN();
    if (l == 0) { bst4(1u, rs, vflag, (NSEG - 3) * fstride); bst4(1u, rs, vflag, (NSEG - 2) * fstride); bst4(1u, rs, vflag, (NSEG - 1) * fstride); }
}

DI void ffn_down(const Params& p, int cu14, LAS unsigned char* L) {
    const int cu = cu14 % 7, grp = cu14 / 7;
    const int tid = threadIdx.x, l = tid & 63, w = __builtin_amdgcn_readfirstlane(tid >> 6), hi = l >> 5, tk = l & 31;
    const rsrc_t rs = __builtin_amdgcn_make_buffer_rsrc(p.ws, 0, (int)WS_END, 0x00020000);
    u32x4 W[2][28];
    {
        LAS unsigned char* stg = L + w * (14 * 16 * 64);
        load_tr<28>(W[0], p.Wd + (size_t)(448 * w) * D + 64 * cu, D, stg, l);
        load_tr<28>(W[1], p.Wd + (size_t)(448 * w) * D + 64 * cu + 32, D, stg, l);
    }
    lds_barrier();
    const int vh = (int)OFF_H + (56 * w) * 512 + l * 16;
    const int vflag = (int)OFF_FLAG_H + (14 * w + min(l, 13)) * 4;
    LAS float* red = (LAS float*)L;
    float* fo = (float*)(p.ws + OFF_FO);
    auto wait_h = [&](int s) -> bool {
        for (int att = 0;; ++att) {
            asm volatile("" ::: "memory");
            const unsigned f = bld4(rs, vflag, s * 256);
            if (!__any(f == 0)) return true;
            if (att > SPIN_CAP) { note_timeout(rs, 0x600 + s); return false; }
            __builtin_amdgcn_s_sleep(8);
        }
    };
    if (!wait_h(grp)) return;
    u32x4 hf[28];
#pragma unroll
    for (int ks = 0; ks < 28; ++ks) hf[ks] = bld16(rs, vh + ks * 1024, grp * (224 * 512));
    for (int s = grp; s < NSEG; s += 2) {
        const int sn = min(s + 2, NSEG - 2 + grp);
        if (!wait_h(sn)) return;
        f32x16 acc[2];
#pragma unroll
        for (int ks = 0; ks < 28; ++ks) {
            if (ks == 0) { mma32_wb<true>(acc[0], hf[0], W[0][0]); mma32_wb<true>(acc[1], hf[0], W[1][0]); }
            else { mma32_wb<false>(acc[0], hf[ks], W[0][ks]); mma32_wb<false>(acc[1], hf[ks], W[1][ks]); }
            SCHED();
            hf[ks] = bld16(rs, vh + ks * 1024, sn * (224 * 512));
            SCHED();
        }
        mma32_done(acc[0], acc[1]);
        lds_barrier();
#pragma unroll
        for (int t = 0; t < 2; ++t)
#pragma unroll
            for (int i = 0; i < 16; ++i) red[((w * 2 + t) * 16 + i) * 64 + l] = acc[t][i];
        lds_barrier();
#pragma unroll
        for (int t = 0; t < 2; ++t)
#pragma unroll
            for (int r = 0; r < 4; ++r) {
                const int i = 4 * w + r;
                const float v = (red[((0 * 2 + t) * 16 + i) * 64 + l] + red[((1 * 2 + t) * 16 + i) * 64 + l]) + (red[((2 * 2 + t) * 16 + i) * 64 + l] + red[((3 * 2 + t) * 16 + i) * 64 + l]);
                bst4(__float_as_uint(v), rs, (int)OFF_FO + ((8 * w + 4 * hi + r) * D + 64 * cu + 32 * t + tk) * 4, s * (32 * D * 4));
            }
        DRAIN();
        if (l == 0) bst4(1u, rs, (int)OFF_FLAG_F + (cu * 4 + w) * 4, s * 128);
    }
}

template <int DATA>
DI bool wait_words(rsrc_t rs, int voff, int soff, unsigned code, bool fast) {
#pragma unroll 1
    for (int att = 0;; ++att) {
        asm volatile("" ::: "memory");
        const unsigned f = bld4(rs, voff, soff);
        if (!__any(f == (DATA ? SENT : 0u))) return true;
        if (att > SPIN_CAP) { note_timeout(rs, code); return false; }
        if (fast) __builtin_amdgcn_s_sleep(2); else __builtin_amdgcn_s_sleep(POLL_SLEEP);
    }
}
#define RST(i)
DI void rows_role(const Params& p, int id, LAS unsigned char* L) {
    const int tid = threadIdx.x, l = tid & 63, w = __builtin_amdgcn_readfirstlane(tid >> 6), r16 = l & 15, q = l >> 4;
    const int grp = id >> 2, qi = id & 3;
    const rsrc_t rs = __builtin_amdgcn_make_buffer_rsrc(p.ws, 0, (int)WS_END, 0x00020000);
    LAS f32x4* red = (LAS f32x4*)(L + XB);
    LAS float* gate = (LAS float*)(L + XB + 4 * 14 * 1024);
    const int h = 4 * qi + w;
    const int k0 = 8 * (r16 >> 2) + (r16 & 3), k1 = k0 + 4;
    const int vz0 = (int)OFF_Z + h * 28 * 1024 + (k0 >> 4) * 14 * 1024 + (q * 16 + (k0 & 15)) * 16, vz1 = (int)OFF_Z + h * 28 * 1024 + (k1 >> 4) * 14 * 1024 + (q * 16 + (k1 & 15)) * 16;
    const int vzm = (int)OFF_ZM + h * 14 * 512 + q * 128 + (r16 & 7) * 16;
    const int mall = (int)OFF_M + wait_parity(rs) * 8192;
    for (int i = 0; i < PRETOUCH; ++i) __builtin_amdgcn_s_sleep(127);
#pragma unroll 1
    for (int sb = 0; sb < NSEG; sb += 16) {
        u32x4 t0[16], t1[16];
#pragma unroll
        for (int k16 = 0; k16 < 16; ++k16) {
            const int sg = sb + k16, u = (id * 4 + w + sg * 37) % 176;
            { const int hh = u / 14, k = u % 14; t0[k16] = bld16(rs, k < 7 ? (int)OFF_U + ((hh * 5 + 4) * D) * 16 + k * 1024 + l * 16 + sg * (16 * 5 * D * 16) : (int)OFF_ZM + hh * 7168 + (k - 7) * 1024 + l * 16 + sg * (16 * 14 * 512), 0); }
            { const int u2 = u + 176, hh = u2 / 14, k = u2 % 14; t1[k16] = bld16(rs, u >= 48 ? OOB : k < 7 ? (int)OFF_U + ((hh * 5 + 4) * D) * 16 + k * 1024 + l * 16 + sg * (16 * 5 * D * 16) : (int)OFF_ZM + hh * 7168 + (k - 7) * 1024 + l * 16 + sg * (16 * 14 * 512), 0); }
        }
#pragma unroll
        for (int k16 = 0; k16 < 16; ++k16) asm volatile("" :: "v"(t0[k16]), "v"(t1[k16]));
    }
    const int b0 = (112 * qi) >> 5, b1 = (112 * qi + 111) >> 5;
    const int rz = 7 * h + min(l & 7, 6);
    const int vmw = l < 8 ? mall + (rz % 56) * 128 + (rz / 56) * 64 : mall + (14 * qi + min(l - 8, 13)) * 128;
#pragma unroll 1
    for (int s = grp; s < NSEG; s += 11) {
        const int pb = grp * 2 + ((s / 11) & 1);
        const bool late = s >= NSEG - 6;
        lds_barrier();
        x_dma(p, s, L, w, l);
        if (!wait_words<0>(rs, (int)OFF_FLAG_ZU + ((l >= 8 && l < 40) ? 112 + ((4 * w + ((l - 8) >> 3)) * 14 + min(b0 + (l & 7), b1)) / 2 : 7 * h + min(l & 7, 6)) * 4, s * 1024, 0x700 + s, false)) return;
        u32x4 z0[14], z1[14], uf[4][7];
#pragma unroll
        for (int ks = 0; ks < 14; ++ks) { z0[ks] = bld16(rs, vz0 + ks * 1024, s * (16 * 28 * 1024)); z1[ks] = bld16(rs, vz1 + ks * 1024, s * (16 * 28 * 1024)); }
#pragma unroll
        for (int hh = 0; hh < 4; ++hh)
#pragma unroll
            for (int t = 0; t < 7; ++t) uf[hh][t] = bld16(rs, (int)OFF_U + (((4 * w + hh) * 5 + q) * D + 112 * qi + 16 * t + r16) * 16, s * (16 * 5 * D * 16));
        DRAIN();
        lds_barrier();
        f32x4 sc[3][2];
#pragma unroll
        for (int i = 0; i < 3; ++i) { sc[i][0] = f32x4{0, 0, 0, 0}; sc[i][1] = sc[i][0]; }
#pragma unroll
        for (int ks = 0; ks < 14; ++ks)
#pragma unroll
            for (int it = 0; it < 2; ++it) {
                const u32x4 xf = *(const LAS u32x4*)(L + (16 * it + r16) * 1024 + (((4 * ks + q) & 48) | (((4 * ks + q) & 15) ^ r16)) * 16);
                sc[0][it] = mfma16(z0[ks], xf, sc[0][it]); sc[1][it] = mfma16(z1[ks], xf, sc[1][it]);
            }
        {
            const int row = tid >> 3, part = tid & 7;
            float g = 0;
#pragma unroll
            for (int k = 0; k < 7; ++k) {
                const int ch = part * 7 + k;
                const u32x4 a = *(const LAS u32x4*)(L + row * 1024 + ((ch & 48) | ((ch & 15) ^ (row & 15))) * 16), b = gld16(p.gw + ch * 8);
#pragma unroll
                for (int e = 0; e < 4; ++e) g += __uint_as_float(a[e] << 16) * __uint_as_float(b[e] << 16) + __uint_as_float(a[e] & 0xFFFF0000u) * __uint_as_float(b[e] & 0xFFFF0000u);
            }
            g += __shfl_xor(g, 1); g += __shfl_xor(g, 2); g += __shfl_xor(g, 4);
            if (part == 0) gate[row] = 1.0f / (1.0f + __expf(-g));
        }
        if (!wait_words<0>(rs, (int)OFF_FLAG_F + min(l, 27) * 4, s * 128, 0xC00 + s, false)) return;
        u32x4 fo[4];
#pragma unroll
        for (int k = 0; k < 4; ++k) { const int u = min(w + 4 * k, 13); fo[k] = bld16(rs, (int)OFF_FO + ((16 * (u & 1) + r16) * D + 112 * qi + 16 * (u >> 1) + 4 * q) * 4, s * (32 * D * 4)); }
        if (!wait_words<1>(rs, vmw, (s + 2) * 16384, 0x800 + s, late)) return;
        RST(0);
        u32x4 zm[14]; u32x2 yf[4][7];
#pragma unroll
        for (int ks = 0; ks < 14; ++ks) zm[ks] = bld16(rs, vzm + ks * 512, s * (16 * 14 * 512));
        SCHED();
#pragma unroll
        for (int ks = 0; ks < 14; ++ks)
#pragma unroll
            for (int it = 0; it < 2; ++it) {
                const u32x4 xf = *(const LAS u32x4*)(L + (16 * it + r16) * 1024 + (((4 * ks + q) & 48) | (((4 * ks + q) & 15) ^ r16)) * 16);
                sc[2][it] = mfma16(zm[ks], xf, sc[2][it]);
            }
#pragma unroll
        for (int it = 0; it < 2; ++it) {
            const int i = 16 * it + r16;
            float v[12];
#pragma unroll
            for (int r = 0; r < 4; ++r) {
                v[r] = (8 * q + r <= i) ? sc[0][it][r] * SC2 : -3.0e38f;
                v[4 + r] = (8 * q + 4 + r <= i) ? sc[1][it][r] * SC2 : -3.0e38f;
                v[8 + r] = q < 2 ? sc[2][it][r] * SC2 : -3.0e38f;
            }
            float mx = v[0];
#pragma unroll
            for (int k = 1; k < 12; ++k) mx = fmaxf(mx, v[k]);
            { const auto x = __builtin_amdgcn_permlane16_swap(__float_as_uint(mx), __float_as_uint(mx), false, false); mx = fmaxf(__uint_as_float(x[0]), __uint_as_float(x[1])); }
            { const auto x = __builtin_amdgcn_permlane32_swap(__float_as_uint(mx), __float_as_uint(mx), false, false); mx = fmaxf(__uint_as_float(x[0]), __uint_as_float(x[1])); }
            float sum = 0;
#pragma unroll
            for (int k = 0; k < 12; ++k) { v[k] = __builtin_amdgcn_exp2f(v[k] - mx); sum += v[k]; }
            { const auto x = __builtin_amdgcn_permlane16_swap(__float_as_uint(sum), __float_as_uint(sum), false, false); sum = __uint_as_float(x[0]) + __uint_as_float(x[1]); }
            { const auto x = __builtin_amdgcn_permlane32_swap(__float_as_uint(sum), __float_as_uint(sum), false, false); sum = __uint_as_float(x[0]) + __uint_as_float(x[1]); }
            const float rl = __builtin_amdgcn_rcpf(sum);
            const u32x4 px = {pk2(v[0] * rl, v[1] * rl), pk2(v[2] * rl, v[3] * rl), pk2(v[4] * rl, v[5] * rl), pk2(v[6] * rl, v[7] * rl)};
            bst16(px, rs, (int)OFF_PX + (h * 2 + it) * 1024 + l * 16, pb * (16 * 2 * 1024));
            const u32x2 pm = {pk2(v[8] * rl, v[9] * rl), pk2(v[10] * rl, v[11] * rl)};
            bst8(pm, rs, (int)OFF_PM + (h * 2 + it) * 512 + l * 8, pb * (16 * 2 * 512));
        }
        RST(1);
        SCHED();
#pragma unroll
        for (int hh = 0; hh < 4; ++hh)
#pragma unroll
            for (int t = 0; t < 7; ++t) yf[hh][t] = bld8(rs, q < 2 ? (int)OFF_U + (((4 * w + hh) * 5 + 4) * D + 112 * qi + 16 * t + r16) * 16 + q * 8 : OOB, s * (16 * 5 * D * 16));
        SCHED();
        asm volatile("s_waitcnt vmcnt(28)" ::: "memory");
        if (l == 0) bst4(1u, rs, (int)OFF_FLAG_P + (qi * 4 + w) * 4, s * 64);
        RST(2);
        if (!wait_words<0>(rs, (int)OFF_FLAG_P + (4 * w + (l & 3)) * 4, s * 64, 0xB00 + s, true)) return;
        RST(3);
        u32x4 px[4][2]; u32x2 pm[4][2];
#pragma unroll
        for (int hh = 0; hh < 4; ++hh)
#pragma unroll
            for (int it = 0; it < 2; ++it) {
                px[hh][it] = bld16(rs, (int)OFF_PX + ((4 * w + hh) * 2 + it) * 1024 + l * 16, pb * (16 * 2 * 1024));
                pm[hh][it] = bld8(rs, (int)OFF_PM + ((4 * w + hh) * 2 + it) * 512 + l * 8, pb * (16 * 2 * 512));
            }
        SCHED();
        f32x4 acc[7][2];
#pragma unroll
        for (int t = 0; t < 7; ++t) { acc[t][0] = f32x4{0, 0, 0, 0}; acc[t][1] = acc[t][0]; }
#pragma unroll
        for (int hh = 0; hh < 4; ++hh)
#pragma unroll
            for (int t = 0; t < 7; ++t)
#pragma unroll
                for (int it = 0; it < 2; ++it) { acc[t][it] = mfma16(uf[hh][t], px[hh][it], acc[t][it]); acc[t][it] = mfma16k16(yf[hh][t], pm[hh][it], acc[t][it]); }
#pragma unroll
        for (int t = 0; t < 7; ++t) { red[(w * 14 + 2 * t) * 64 + l] = acc[t][0]; red[(w * 14 + 2 * t + 1) * 64 + l] = acc[t][1]; }
        RST(4);
        lds_barrier();
#pragma unroll
        for (int k = 0; k < 4; ++k) {
            const int u = w + 4 * k;
            if (u < 14) {
                const f32x4 a = (red[(0 * 14 + u) * 64 + l] + red[(1 * 14 + u) * 64 + l]) + (red[(2 * 14 + u) * 64 + l] + red[(3 * 14 + u) * 64 + l]);
                const int i = 16 * (u & 1) + r16, f = 112 * qi + 16 * (u >> 1) + 4 * q;
                const float g = gate[i];
                const u32x4 bb = fo[k];
                const u32x2 o = {pk2(g * a[0] + (1.f - g) * __uint_as_float(bb[0]), g * a[1] + (1.f - g) * __uint_as_float(bb[1])), pk2(g * a[2] + (1.f - g) * __uint_as_float(bb[2]), g * a[3] + (1.f - g) * __uint_as_float(bb[3]))};
                *(GAS u32x2*)(p.out + ((size_t)s * 32 + i) * D + f) = o;
            }
        }
        RST(5);
    }
}

__global__ void __launch_bounds__(256, 1) k_main(Params p) {
    extern __shared__ __attribute__((aligned(16))) unsigned char smem[];
    LAS unsigned char* L = (LAS unsigned char*)smem;
    const int b = blockIdx.x, xl = b & 7, idx = (b >> 3) * 4 + (xl & 3);
    if (xl < 4) {
        if (idx < 112) chain_merged(p, idx, L); else chain_mx(p, idx - 112, L);
    } else if (!(p.pad & 1) || idx >= 84) {
        if (idx < 28) stream_x<0>(p, idx, L);
        else if (idx < 56) stream_x<1>(p, idx - 28, L);
        else if (idx < 70) { for (int i = 0; i < FFN_DELAY; ++i) __builtin_amdgcn_s_sleep(127); stream_x<2>(p, idx - 56, L); }
        else if (idx < 84) { for (int i = 0; i < FFN_DELAY; ++i) __builtin_amdgcn_s_sleep(127); ffn_down(p, idx - 70, L); }
        else { if (idx == 127) probe_parity(p); if (!(p.pad & 3)) rows_role(p, idx - 84, L); }
    }
}

#define TAU_M 850
#define TAU_S 800
#define TAU_X 2500
int g_tauM = TAU_M, g_tauS = TAU_S, g_tauX = TAU_X, g_dbg = 0;

extern "C" void kernel_launch(void* const* d_in, const int* in_sizes, int n_in, void* d_out, int out_size, void* d_ws, size_t ws_size, hipStream_t stream) {
    Params p;
    p.x = (const u16*)d_in[0]; p.mem0 = (const u16*)d_in[1]; p.Wq = (const u16*)d_in[2]; p.Wk = (const u16*)d_in[3]; p.Wv = (const u16*)d_in[4]; p.Wo = (const u16*)d_in[5];
    p.Wg = (const u16*)d_in[8]; p.Wu = (const u16*)d_in[9]; p.Wd = (const u16*)d_in[10]; p.gw = (const u16*)d_in[11];
    p.ws = (unsigned char*)(((uintptr_t)d_ws + 2 * MB - 1) & ~(uintptr_t)(2 * MB - 1));
    p.out = (u16*)d_out;
    p.tauM = g_tauM; p.tauS = g_tauS; p.tauX = 384; p.tauXm = g_tauX; p.pad = g_dbg; p.pad2 = 0;
    constexpr int LDS_MAIN = 2 * XB + 4 * 14 * 16 * 64;
    hipFuncSetAttribute((const void*)k_main, hipFuncAttributeMaxDynamicSharedMemorySize, LDS_MAIN);
    hipFuncSetAttribute((const void*)k_prep, hipFuncAttributeMaxDynamicSharedMemorySize, 65536);
    k_prep<<<NPREP, 256, 65536, stream>>>(p);
    k_main<<<256, 256, LDS_MAIN, stream>>>(p);

}
```

```cpp
#include <hip/hip_runtime.h>
#include <stdint.h>

#define DI __device__ __forceinline__
#define LAS __attribute__((address_space(3)))
#define GAS __attribute__((address_space(1)))
typedef unsigned short u16;
typedef __bf16 bf16x8 __attribute__((ext_vector_type(8)));
typedef short s16x4 __attribute__((ext_vector_type(4)));
typedef float f32x4 __attribute__((ext_vector_type(4)));
typedef float f32x16 __attribute__((ext_vector_type(16)));
typedef unsigned u32x4 __attribute__((ext_vector_type(4)));
typedef unsigned u32x2 __attribute__((ext_vector_type(2)));
typedef __amdgpu_buffer_rsrc_t rsrc_t;

constexpr int D = 448, NH = 16, NSEG = 64, FF = 1792;
constexpr size_t MB = 1u << 20;
constexpr size_t OFF_RING = 0;
constexpr size_t OFF_M = 576 * 1024;
constexpr size_t OFF_PROBE = 1664 * 1024;
constexpr size_t OFF_CTRL = 2 * MB;
constexpr size_t OFF_FLAG_ZU = OFF_CTRL + 4096;
constexpr size_t OFF_FLAG_H = OFF_FLAG_ZU + 64 * 256 * 4;
constexpr size_t OFF_FLAG_F = OFF_FLAG_H + 64 * 64 * 4;
constexpr size_t OFF_FLAG_P = OFF_FLAG_F + 64 * 32 * 4;
constexpr size_t CTRL_END = OFF_FLAG_P + 64 * 16 * 4;
constexpr size_t OFF_STAT = 2 * MB + 512 * 1024;
constexpr size_t SZ_MAT = (size_t)NH * D * D * 2;
constexpr size_t OFF_A = 3 * MB;
constexpr size_t OFF_AT = OFF_A + 7 * MB;
constexpr size_t OFF_BT = OFF_AT + 7 * MB;
constexpr size_t OFF_Z = OFF_BT + 7 * MB;
constexpr size_t OFF_U = OFF_Z + 28 * MB;
constexpr size_t OFF_ZM = OFF_U + 35 * MB;
constexpr size_t OFF_H = OFF_ZM + 7 * MB;
constexpr size_t OFF_FO = OFF_H + 7 * MB;
constexpr size_t OFF_PX = OFF_FO + 4 * MB;
constexpr size_t OFF_PM = OFF_PX + 2 * MB;
constexpr size_t WS_END = OFF_PM + 1 * MB;
static_assert(WS_END + 2 * MB <= 256 * MB, "workspace");

constexpr int AUX_SC1 = 16;
constexpr unsigned SENT = 0xFFFFFFFFu;
constexpr float SC2 = 0.0625f * 1.4426950408889634f;

struct Params {
    const u16* x; const u16* mem0; const u16* Wq; const u16* Wk; const u16* Wv; const u16* Wo;
    const u16* Wg; const u16* Wu; const u16* Wd; const u16* gw;
    unsigned char* ws; u16* out;
    int tauM, tauS, tauX, tauXm, pad, pad2;
};

DI unsigned pk2(float a, float b) { typedef __bf16 bf2 __attribute__((ext_vector_type(2))); bf2 v = {(__bf16)a, (__bf16)b}; return __builtin_bit_cast(unsigned, v); }
DI f32x4 mfma16(u32x4 a, u32x4 b, f32x4 c) { return __builtin_amdgcn_mfma_f32_16x16x32_bf16(__builtin_bit_cast(bf16x8, a), __builtin_bit_cast(bf16x8, b), c, 0, 0, 0); }
DI f32x4 mfma16k16(u32x2 a, u32x2 b, f32x4 c) { return __builtin_amdgcn_mfma_f32_16x16x16bf16_1k(__builtin_bit_cast(s16x4, a), __builtin_bit_cast(s16x4, b), c, 0, 0, 0); }
DI f32x16 mfma32(u32x4 a, u32x4 b, f32x16 c) { return __builtin_amdgcn_mfma_f32_32x32x16_bf16(__builtin_bit_cast(bf16x8, a), __builtin_bit_cast(bf16x8, b), c, 0, 0, 0); }
template <bool FIRST> DI void mma32_wa(f32x16& acc, const u32x4& w, const u32x4& x) {
    if (FIRST) asm volatile("v_mfma_f32_32x32x16_bf16 %0, %1, %2, 0" : "=&v"(acc) : "a"(w), "v"(x));
    else asm volatile("v_mfma_f32_32x32x16_bf16 %0, %1, %2, %0" : "+v"(acc) : "a"(w), "v"(x));
}
template <bool FIRST> DI void mma32_wb(f32x16& acc, const u32x4& x, const u32x4& w) {
    if (FIRST) asm volatile("v_mfma_f32_32x32x16_bf16 %0, %1, %2, 0" : "=&v"(acc) : "v"(x), "a"(w));
    else asm volatile("v_mfma_f32_32x32x16_bf16 %0, %1, %2, %0" : "+v"(acc) : "v"(x), "a"(w));
}
DI void mma32_done(f32x16& a, f32x16& b) { asm volatile("s_nop 7\n\ts_nop 3" : "+v"(a), "+v"(b)); }
DI u32x4 gld16(const void* p) { return *(const GAS u32x4*)p; }
DI u32x4 bld16(rsrc_t r, int voff, int soff) { return __builtin_amdgcn_raw_buffer_load_b128(r, voff, soff, AUX_SC1); }
DI u32x2 bld8(rsrc_t r, int voff, int soff) { return __builtin_amdgcn_raw_buffer_load_b64(r, voff, soff, AUX_SC1); }
DI unsigned bld4(rsrc_t r, int voff, int soff) { return __builtin_amdgcn_raw_buffer_load_b32(r, voff, soff, AUX_SC1); }
DI void bst16(u32x4 v, rsrc_t r, int voff, int soff) { __builtin_amdgcn_raw_buffer_store_b128(v, r, voff + soff, 0, AUX_SC1); }
DI void bst8(u32x2 v, rsrc_t r, int voff, int soff) { __builtin_amdgcn_raw_buffer_store_b64(v, r, voff, soff, AUX_SC1); }
DI void bst4(unsigned v, rsrc_t r, int voff, int soff) { __builtin_amdgcn_raw_buffer_store_b32(v, r, voff, soff, AUX_SC1); }
DI unsigned ct() { return (unsigned)__builtin_amdgcn_s_memtime(); }
DI void wait_until(unsigned t) { while ((int)(ct() - t) < 0) {} }
DI unsigned rt16() { return (unsigned)__builtin_amdgcn_s_memrealtime() << 4; }
DI void wait_slot(unsigned slot) { while ((int)(rt16() - slot) < 0) {} }
DI unsigned slot_max(const LAS unsigned char* w) {
    const u32x4 v = *(const LAS u32x4*)w;
    const unsigned a = (int)(v.x - v.y) > 0 ? v.x : v.y, b = (int)(v.z - v.w) > 0 ? v.z : v.w;
    return __builtin_amdgcn_readfirstlane((int)(a - b) > 0 ? a : b);
}
DI unsigned next_slot(unsigned slot, unsigned q) { const unsigned now = rt16(); do slot += q; while ((int)(now - slot) >= 0); return slot; }
template <int CTRL> DI float dpp(float v) { return __int_as_float(__builtin_amdgcn_update_dpp(0, __float_as_int(v), CTRL, 0xf, 0xf, true)); }
constexpr int ROR1 = 0x121, ROR2 = 0x122, ROR4 = 0x124, ROR8 = 0x128, QP0022 = 0xA0, QP1133 = 0xF5;
DI unsigned mx4(u32x4 v) { return max(max(v.x, v.y), max(v.z, v.w)); }
DI void lds_barrier() { asm volatile("s_waitcnt lgkmcnt(0)" ::: "memory"); __builtin_amdgcn_s_barrier(); asm volatile("" ::: "memory"); }
#define SCHED() __builtin_amdgcn_sched_barrier(0)
#define DRAIN() asm volatile("s_waitcnt vmcnt(0)" ::: "memory")
constexpr int SPIN_CAP = 1 << 18;
#define PRETOUCH 1
#define POLL_SLEEP 32
DI void note_timeout(rsrc_t rs, unsigned code) { bst4(code, rs, (int)OFF_CTRL + 64, 0); }

DI int pidx(int i, int r) { return 32 * (i >> 1) + 8 * (r >> 2) + 4 * (i & 1) + (r & 3); }

constexpr int NPREP = 512;
#define PREP_SC1 1
#define WARM 0
DI size_t piece(int h, int blk, int oct, int pos) { return ((((size_t)h * 14 + blk) * 56 + oct) * 32 + pos) * 16; }

__global__ void __launch_bounds__(256, 2) k_prep(Params p) {
    extern __shared__ __attribute__((aligned(16))) unsigned char smem[];
    const int tid = threadIdx.x, l = tid & 63, w = __builtin_amdgcn_readfirstlane(tid >> 6), r16 = l & 15, q = l >> 4;
    const int blk = blockIdx.x;
#define PSTAMP(x)
    {
        if (w == 0) {
#pragma unroll
            for (int n = 0; n < 2; ++n) {
                const int i = (blk * 2 + n) * 64 + l, fb = i >> 10, o = i & 511;
                u32x4 v = {SENT, SENT, SENT, SENT};
                if ((fb < 2 || fb == 8) && o < 448) { const int b = o >> 3, t = o & 7; v = gld16(p.mem0 + t * D + 8 * b); v.x = min(v.x, SENT - 1); v.y = min(v.y, SENT - 1); v.z = min(v.z, SENT - 1); v.w = min(v.w, SENT - 1); }
                if (fb < 36) *(GAS u32x4*)(p.ws + OFF_RING + (size_t)i * 16) = v;
            }
        } else if (w & 1) {
            constexpr int NW = (int)((CTRL_END - OFF_CTRL) / 4);
            static_assert(NW <= 2 * NPREP * 64, "flags");
            const int i = (blk * 2 + (w >> 1)) * 64 + l;
            if (i < NW && (!(p.pad & 1) || i >= (int)((OFF_FLAG_F - OFF_CTRL) / 4) || i < 16)) ((GAS unsigned*)(p.ws + OFF_CTRL))[i] = 0;
        } else if (w == 2) {
#pragma unroll
            for (int n = 0; n < 3; ++n) {
                const int i = (blk * 3 + n) * 64 + l, o = i & 511;
                u32x4 v = {SENT, SENT, SENT, SENT};
                if (i < 1024 && o < 448) v = gld16(p.mem0 + (o & 7) * D + 8 * (o >> 3));
                if (i < 67 * 1024) *(GAS u32x4*)(p.ws + OFF_M + (size_t)i * 16) = v;
            }
        }
        PSTAMP(t1);
        const int T = blk >> 3;
        const bool tA = T < 32;
        const int h = 2 * (blk & 7) + ((T >> 4) & 1), tr = (T >> 2) & 3, tc = T & 3;
        const int wr = w >> 1, wc = w & 1, rt = 2 * tr + wr, ctl = 2 * tc + wc;
        const bool active = rt < 7 && ctl < 7;
        const bool corner = tA && tr == 3 && tc == 3;
        if (corner && (w == 1 || w == 2)) {
            const int wv = ((blk & 7) * 2 + (T >> 4)) * 2 + w - 1;
            const rsrc_t rs = __builtin_amdgcn_make_buffer_rsrc(p.ws, 0, (int)WS_END, 0x00020000);
            unsigned xcc; asm volatile("s_getreg_b32 %0, hwreg(HW_REG_XCC_ID)" : "=s"(xcc));
            const int po = (int)OFF_PROBE + min(l, 5) * 8192 + wv * 128;
            bst4(0u, rs, po, 0);
            DRAIN();
            unsigned best0 = 1u << 20, best1 = 1u << 20;
#pragma unroll 1
            for (int k = 0; k < 6; ++k) {
                const int o = __builtin_amdgcn_readlane(po, k);
                unsigned v; unsigned long long c0, c1;
                asm volatile("s_memtime %0\n\ts_waitcnt lgkmcnt(0)\n\tbuffer_load_dword %2, %3, %4, 0 offen sc1\n\ts_waitcnt vmcnt(0)\n\ts_memtime %1\n\ts_waitcnt lgkmcnt(0)"
                             : "=&s"(c0), "=&s"(c1), "=&v"(v) : "v"(o), "s"(rs) : "memory");
                const unsigned dt = (unsigned)(c1 - c0);
                if (k & 1) best1 = min(best1, dt); else best0 = min(best0, dt);
            }
            if (l == 0) *(GAS u32x2*)(p.ws + OFF_PROBE + 65536 + wv * 8) = (xcc & 15) < 4 ? u32x2{best0, best1} : u32x2{best1, best0};
            return;
        }
        const int wd = corner ? w / 3 : w;
        LAS unsigned char* L = (LAS unsigned char*)smem;
        const char* baseR = (const char*)(tA ? p.Wq : p.Wv); const char* baseC = (const char*)(tA ? p.Wk : p.Wo);
        unsigned offR[4], offC[4];
#pragma unroll
        for (int n = 0; n < 4; ++n) {
            const int rho = 8 * (4 * wd + n) + (l >> 3), ch = (l & 7) ^ (rho & 7);
            const int rr = min(128 * tr + 64 * (rho >> 6) + pidx((rho & 63) >> 4, rho & 15), D - 1);
            offR[n] = ((rr * 16 + h) * 256 + ch * 8) * 2;
            if (tA) { const int cc = min(128 * tc + 64 * (rho >> 6) + pidx((rho & 63) >> 4, rho & 15), D - 1); offC[n] = ((cc * 16 + h) * 256 + ch * 8) * 2; }
            else { const int e = 4 * (4 * w + n) + (l >> 4), c16 = (l & 15) ^ ((e & 7) << 1); offC[n] = ((h * 256 + e) * D + min(128 * tc + 8 * c16, D - 8)) * 2; }
        }
        auto dma = [&](int kc) __attribute__((always_inline)) {
            LAS unsigned char* b = L + (kc & 1) * 32768;
            const char* bR = baseR + 128 * kc; const char* bC = baseC + (tA ? 128 * kc : 128 * kc * D);
#pragma unroll
            for (int n = 0; n < 4; ++n) {
                __builtin_amdgcn_global_load_lds((const GAS unsigned*)(bR + offR[n]), (LAS unsigned*)(b + (4 * wd + n) * 1024), 16, 0, 0);
                __builtin_amdgcn_global_load_lds((const GAS unsigned*)(bC + offC[n]), (LAS unsigned*)(b + 16384 + (4 * wd + n) * 1024), 16, 0, 0);
            }
        };
        f32x4 a1[4][4], a2[4][4];
#pragma unroll
        for (int i = 0; i < 4; ++i)
#pragma unroll
            for (int j = 0; j < 4; ++j) { a1[i][j] = f32x4{0, 0, 0, 0}; a2[i][j] = a1[i][j]; }
        dma(0); dma(1);
#pragma unroll 1
        for (int kc = 0; kc < 4; ++kc) {
            if (kc < 3) asm volatile("s_waitcnt vmcnt(8)" ::: "memory"); else DRAIN();
            lds_barrier();
            const LAS unsigned char* b = L + (kc & 1) * 32768;
            if (active) {
#pragma unroll
                for (int k2 = 0; k2 < 2; ++k2) {
                    u32x4 fa[4], fb[4];
#pragma unroll
                    for (int i = 0; i < 4; ++i) { const int rho = 64 * wr + 16 * i + r16; fa[i] = *(const LAS u32x4*)(b + rho * 128 + ((4 * k2 + q) ^ (rho & 7)) * 16); }
                    if (tA) {
#pragma unroll
                        for (int j = 0; j < 4; ++j) { const int rho = 64 * wc + 16 * j + r16; fb[j] = *(const LAS u32x4*)(b + 16384 + rho * 128 + ((4 * k2 + q) ^ (rho & 7)) * 16); }
#pragma unroll
                        for (int i = 0; i < 4; ++i)
#pragma unroll
                            for (int j = 0; j < 4; ++j) { a1[i][j] = mfma16(fa[i], fb[j], a1[i][j]); a2[j][i] = mfma16(fb[j], fa[i], a2[j][i]); }
                    } else {
#pragma unroll
                        for (int j = 0; j < 4; ++j) {
                            const int e0 = 32 * k2 + 8 * q + (r16 >> 2), c16 = 8 * wc + 2 * j + ((l & 3) >> 1);
                            const s16x4 lo = __builtin_amdgcn_ds_read_tr16_b64_v4i16((LAS s16x4*)(b + 16384 + e0 * 256 + (c16 ^ ((e0 & 7) << 1)) * 16 + (l & 1) * 8));
                            const s16x4 hi = __builtin_amdgcn_ds_read_tr16_b64_v4i16((LAS s16x4*)(b + 16384 + (e0 + 4) * 256 + (c16 ^ (((e0 + 4) & 7) << 1)) * 16 + (l & 1) * 8));
                            const u32x2 x = __builtin_bit_cast(u32x2, lo), y = __builtin_bit_cast(u32x2, hi);
                            fb[j] = u32x4{x.x, x.y, y.x, y.y};
                        }
#pragma unroll
                        for (int i = 0; i < 4; ++i)
#pragma unroll
                            for (int j = 0; j < 4; ++j) a1[i][j] = mfma16(fa[i], fb[j], a1[i][j]);
                    }
                }
            }
            if (kc < 2) { lds_barrier(); dma(kc + 2); }
        }
        PSTAMP(t2);
        const rsrc_t rs = __builtin_amdgcn_make_buffer_rsrc(p.ws, 0, (int)WS_END, 0x00020000);
#define PST(v, off) bst16(v, rs, (int)(off), 0)
        if (active) {
#pragma unroll
            for (int P = 0; P < 2; ++P)
#pragma unroll
                for (int j = 0; j < 4; ++j) {
                    const u32x4 v1 = {pk2(a1[2 * P][j][0], a1[2 * P][j][1]), pk2(a1[2 * P][j][2], a1[2 * P][j][3]), pk2(a1[2 * P + 1][j][0], a1[2 * P + 1][j][1]), pk2(a1[2 * P + 1][j][2], a1[2 * P + 1][j][3])};
                    PST(v1, (tA ? OFF_AT : OFF_BT) + piece(h, 2 * ctl + (j >> 1), 8 * rt + 4 * P + q, 16 * (j & 1) + r16));
                    if (tA) {
                        const u32x4 v2 = {pk2(a2[2 * P][j][0], a2[2 * P][j][1]), pk2(a2[2 * P][j][2], a2[2 * P][j][3]), pk2(a2[2 * P + 1][j][0], a2[2 * P + 1][j][1]), pk2(a2[2 * P + 1][j][2], a2[2 * P + 1][j][3])};
                        PST(v2, OFF_A + piece(h, 2 * rt + (j >> 1), 8 * ctl + 4 * P + q, 16 * (j & 1) + r16));
                    }
                }
        }
    }
}

DI int s_phys(int o) { return (o >> 13) * 16384 + (o & 8191); }
DI int s_piece(int h, int pc, int m) { return s_phys((((m >> 2) * 4 + (h >> 2)) * 3 + (pc >> 2)) * 1024 + (pc & 3) * 256 + (h & 3) * 64 + (m & 3) * 16); }
constexpr int SLOT = 9 * 16384, MCOPY = 16384, SOFF = 2 * 16384, SCOPY = 3 * 16384, MXOFF = 8 * 16384;
constexpr int L_M = 0;
constexpr int L_ZERO = 7168;
constexpr int L_S = 8192;
constexpr int L_O = 9216;
constexpr int L_X = 10240;
constexpr int L_DUMMY = 14336;
constexpr int L_T = 15360;

DI bool bad2(u32x4 a, u32x4 b) { return __any(max(mx4(a), mx4(b)) == SENT); }

DI int ring_parity(const Params& p) {
    const int l = threadIdx.x & 63;
    const u32x2 v = *(const GAS u32x2*)(p.ws + OFF_PROBE + 65536 + (l & 31) * 8);
    unsigned a = v.x, b = v.y;
#pragma unroll
    for (int m = 32; m >= 1; m >>= 1) { a = min(a, (unsigned)__shfl_xor((int)a, m)); b = min(b, (unsigned)__shfl_xor((int)b, m)); }
    return __builtin_amdgcn_readfirstlane(b < a ? 1 : 0);
}

#define VLD16(dst, voff, soff) asm volatile("buffer_load_dwordx4 %0, %1, %2, %3 offen sc1" : "=v"(dst) : "v"(voff), "s"(rs), "s"(soff) : "memory")
#define VLD4(dst, voff, soff) asm volatile("buffer_load_dword %0, %1, %2, %3 offen sc1" : "=v"(dst) : "v"(voff), "s"(rs), "s"(soff) : "memory")
#define VST4(val, voff, soff) asm volatile("buffer_store_dword %0, %1, %2, %3 offen sc1" :: "v"(val), "v"(voff), "s"(rs), "s"(soff) : "memory")
#define VST8(val, voff, soff) asm volatile("buffer_store_dwordx2 %0, %1, %2, %3 offen sc1" :: "v"(val), "v"(voff), "s"(rs), "s"(soff) : "memory")
#define MM_AV0(d, a, b) asm volatile("v_mfma_f32_16x16x32_bf16 %0, %1, %2, 0" : "=&v"(d) : "a"(a), "v"(b))
#define MM_AV(d, a, b) asm volatile("v_mfma_f32_16x16x32_bf16 %0, %1, %2, %0" : "+v"(d) : "a"(a), "v"(b))
#define MM_VA0(d, a, b) asm volatile("v_mfma_f32_16x16x32_bf16 %0, %1, %2, 0" : "=&v"(d) : "v"(a), "a"(b))
#define MM_VA(d, a, b) asm volatile("v_mfma_f32_16x16x32_bf16 %0, %1, %2, %0" : "+v"(d) : "v"(a), "a"(b))
constexpr int OOB = 0x7FFFFF00;
#define REPL 3
#define FFN_DELAY 1
#define MODE 0
#define ADAPT_UP 0
#define ADAPT_DN 0
#define XUP 96
#define XDN 6
#define WARM_CODE 1
#define EXTRA 0
#define RDELAY 3
#define XCLAMP 400
#define RAMP 64
#define RAMPN 10
#define LPT(k, idx, base)
#define VWAIT() do { SCHED(); asm volatile("s_waitcnt vmcnt(0)" ::: "memory"); SCHED(); } while (0)
template <int V> struct IntC { static constexpr int value = V; };

DI void chain_merged(const Params& p, int role, LAS unsigned char* L) {
    const int tid = threadIdx.x, l = tid & 63, w = __builtin_amdgcn_readfirstlane(tid >> 6), r16 = l & 15, q = l >> 4;
    const int h = role / 7, c = role % 7, g = role / 56, j = role % 56;
    const int parl = (l >> 3) & 1;
    const rsrc_t rs = __builtin_amdgcn_make_buffer_rsrc(p.ws, 0, (int)WS_END, 0x00020000);
    u32x4 AT[4][4], AZ[14], BT[2][14];
    {
#pragma unroll
        for (int tt = 0; tt < 4; ++tt)
#pragma unroll
            for (int kk = 0; kk < 4; ++kk) {
                const int ks = min(4 * w + kk, 13);
                AT[tt][kk] = gld16(p.ws + OFF_AT + piece(h, 2 * c + (tt >> 1), 4 * ks + q, 16 * (tt & 1) + r16));
            }
#pragma unroll
        for (int u = 0; u < 2; ++u)
#pragma unroll
            for (int ks = 0; ks < 14; ++ks) BT[u][ks] = gld16(p.ws + OFF_BT + piece(4 * w + 2 * u + parl, j >> 2, 4 * ks + q, 8 * (j & 3) + (l & 7)));
#pragma unroll
        for (int ks = 0; ks < 14; ++ks) AZ[ks] = gld16(p.ws + OFF_A + piece(h, 2 * c + (w >> 1), 4 * ks + q, 16 * (w & 1) + r16));
        SCHED();
#pragma unroll
        for (int tt = 0; tt < 4; ++tt)
#pragma unroll
            for (int kk = 0; kk < 4; ++kk) {
                if (4 * w + kk > 13) AT[tt][kk] = u32x4{0, 0, 0, 0};
                asm volatile("" : "+a"(AT[tt][kk]));
            }
#pragma unroll
        for (int ks = 0; ks < 14; ++ks) asm volatile("" : "+a"(AZ[ks]), "+a"(BT[0][ks]), "+a"(BT[1][ks]));
    }
    const unsigned ONE = 0x3F80u;
    u32x4 SEL0, SEL1; u32x2 AONE, AW; f32x4 CINIT;
    {
#pragma unroll
        for (int n = 0; n < 4; ++n) { const unsigned v = (r16 == 2 * n ? ONE : 0u) | (r16 == 2 * n + 1 ? ONE << 16 : 0u); SEL0[n] = v; SEL1[n] = q < 3 ? v : 0u; }
        if (q == 3 && r16 == 8) { SEL1[0] = ONE | (ONE << 16); SEL1[1] = ONE; }
        AONE = u32x2{ONE | (ONE << 16), ONE | (ONE << 16)};
        AW = u32x2{q == 2 ? ONE : 0u, 0u};
        const float NI = -3.0e38f;
        CINIT = q < 2 ? f32x4{0, 0, 0, 0} : (q == 2 ? f32x4{0, NI, NI, NI} : f32x4{NI, NI, NI, NI});
    }
    if (tid < 64) *(LAS u32x4*)(L + L_ZERO + tid * 16) = u32x4{0, 0, 0, 0};
    const int ring = (int)OFF_RING + ring_parity(p) * 8192;
    const int lo_m = (4 * parl + q) * 128 + (l & 7) * 16;
    const int vm0 = ring + min(2 * w, 6) * 1024 + lo_m, vm1 = w < 3 ? ring + (2 * w + 1) * 1024 + lo_m : OOB;
    const int vs0 = ring + SOFF + s_phys(((g * 4 + w) * 3 + 0) * 1024 + l * 16), vs1 = ring + SOFF + s_phys(((g * 4 + w) * 3 + 1) * 1024 + l * 16), vs2 = ring + SOFF + s_phys(((g * 4 + w) * 3 + 2) * 1024 + l * 16);
    const int vspub = ring + SOFF + (l & 1) * SCOPY + s_piece(h, c, l >> 3) + ((l & 7) >> 1) * 4;
    const int m4 = (l >> 3) & 3, f8 = l & 7;
    const bool toring = (f8 & 1) == 0;
    const int vmring = ring + (l >> 5) * MCOPY + j * 128 + (4 * g + m4) * 16 + (f8 >> 1) * 4;
    const int vmglob = (int)OFF_M + (ring - (int)OFF_RING) + j * 128 + (4 * g + m4) * 16 + (f8 >> 1) * 4;
    const int vmx = ring + MXOFF + j * 128 + (4 * g + m4) * 16 + (f8 >> 1) * 4;
    const bool tomx = !toring && l >= 32;
    const int vra0 = vspub, vra1 = toring ? vmring : (tomx ? vmx : OOB);
    const int vu0 = (int)OFF_U + (((4 * w + parl) * 5 + q) * D + 8 * j + (l & 7)) * 16, vu1 = vu0 + 2 * 5 * D * 16;
    const int vy0 = l < 32 ? (int)OFF_U + (((4 * w + parl) * 5 + 4) * D + 8 * j + (l & 7)) * 16 + (q & 1) * 8 : OOB, vy1 = vy0 == OOB ? OOB : vy0 + 2 * 5 * D * 16;
    const int vz = parl ? OOB : (int)OFF_ZM + (h * 14 + 2 * c + (w >> 1)) * 512 + q * 128 + (l & 7) * 16 + (w & 1) * 8;
    const int vflag = (int)OFF_FLAG_ZU + (112 + ((4 * w + (l & 3)) * 14 + (j >> 2)) / 2) * 4;
    const LAS unsigned char* a0p = L + (parl ? L_ZERO : L_M + c * 1024) + l * 16;
    const LAS unsigned char* a1p = L + (parl ? L_M + c * 1024 : L_ZERO) + l * 16;
    const int hl = (l >> 2) & 3;
    const unsigned long long hvm = __builtin_amdgcn_ballot_w64((hl & 1) == (q >> 1)), hbm = __builtin_amdgcn_ballot_w64(hl >= 2), h1m = __builtin_amdgcn_ballot_w64((hl & 1) != 0);
    const unsigned SENTV = SENT;
    lds_barrier();
    unsigned tref = ct();
    unsigned slot = MODE == 1 ? (rt16() / p.tauX + 1) * p.tauX : 0;
    int tauM = p.tauM, tauS = p.tauS;
    u32x2 YA0 = {0, 0}, YA1 = {0, 0};
#define STAMP(i, from)
#define COUNT(i, v)
    const unsigned long long parm = __builtin_amdgcn_ballot_w64(parl != 0);
    LAS unsigned char* const ldm0 = L + L_M + (2 * w) * 1024 + l * 16;
    LAS unsigned char* const ldm1 = L + (w < 3 ? L_M + (2 * w + 1) * 1024 : L_DUMMY) + l * 16;
    const unsigned lds_sa = (unsigned)(size_t)(L + L_S + w * 256 + ((l & 7) * 8 + 4 * (q & 1) + 2 * parl + (l >> 5)) * 4);
    const unsigned lds_oa = (unsigned)(size_t)(L + L_O + w * 128 + ((l & 3) * 8 + 4 * (q & 1) + 2 * (hl & 1) + (l >> 5)) * 4);
    const unsigned a0a = (unsigned)(size_t)a0p, a1a = (unsigned)(size_t)a1p;
    const int mc0 = ((REPL & 1) && ((role >> 2) & 1)) ? MCOPY : 0, sc0 = ((REPL & 2) && ((role >> 2) & 1)) ? SCOPY : 0;
    u32x4 mf[7];
    const int nst = 1 + (g == 0 ? 2 : 0) + (w == 1 ? 2 : 0);
    u32x2 ZS = {0, 0};
    auto tail = [&]() __attribute__((always_inline)) {
        f32x4 ze, zo;
        asm volatile("v_mfma_f32_16x16x32_bf16 %0, %2, %4, 0\n\tv_mfma_f32_16x16x32_bf16 %1, %3, %4, 0" : "=&v"(ze), "=&v"(zo) : "a"(AZ[0]), "a"(AZ[1]), "v"(mf[0]));
#pragma unroll
        for (int i = 1; i < 7; ++i)
            asm volatile("v_mfma_f32_16x16x32_bf16 %0, %2, %4, %0\n\tv_mfma_f32_16x16x32_bf16 %1, %3, %4, %1" : "+v"(ze), "+v"(zo) : "a"(AZ[2 * i]), "a"(AZ[2 * i + 1]), "v"(mf[i]));
        asm volatile("s_nop 7" : "+v"(ze), "+v"(zo));
        float z[4];
#pragma unroll
        for (int r = 0; r < 4; ++r) z[r] = ze[r] + dpp<ROR8>(zo[r]);
        ZS = u32x2{pk2(z[0], z[1]), pk2(z[2], z[3])};
        asm volatile("" : "+v"(ZS));
    };
    auto stores = [&](int t) __attribute__((always_inline)) {
        const int vz_ = vz, vra0_ = vra0, vra1_ = vra1, vy0_ = vy0, vy1_ = vy1; const unsigned sent_ = SENTV;
        VST8(ZS, vz_, t * (16 * 14 * 512));
        if (g == 0) { VST8(YA0, vy0_, t * (16 * 5 * D * 16)); VST8(YA1, vy1_, t * (16 * 5 * D * 16)); }
        if (w == 1) { const int sp = ((t - 1) & 3) * SLOT; VST4(sent_, vra0_, sp); VST4(sent_, vra1_, sp); }
    };
    int cap = SPIN_CAP;
    const unsigned lsum_s = (unsigned)(size_t)(L + L_S) + l * 4, lsum_o = (unsigned)(size_t)(L + L_O) + (l & 31) * 4;
    const unsigned ldm0a = (unsigned)(size_t)ldm0, ldm1a = (unsigned)(size_t)ldm1;
    unsigned flg;
    asm volatile("buffer_load_dword %0, %1, %2, 0 offen sc1\n\ts_waitcnt vmcnt(0)" : "=&v"(flg) : "v"(vflag), "s"(rs) : "memory");
#pragma unroll 1
    for (int s = 0; s < NSEG; ++s) {
        const int so = (s & 3) * SLOT, sU = s * (16 * 5 * D * 16);
        if (s > 0) wait_until(tref + tauM);
        STAMP(0, tref);
        u32x4 m0, m1;
        VLD16(m0, vm0, so + mc0); VLD16(m1, vm1, so + mc0);
        VWAIT();
        f32x4 te2, to2, te3, to3; u32x4 B0;
        unsigned chk;
#define W_HEAD() asm volatile( \
            "v_mfma_f32_16x16x32_bf16 v[208:211], %[a00], %[m0], 0\n\tv_mfma_f32_16x16x32_bf16 v[212:215], %[a01], %[m0], 0\n\t" \
            "ds_write_b128 %[l0], %[m0]\n\t" \
            "v_mfma_f32_16x16x32_bf16 v[208:211], %[a02], %[m1], v[208:211]\n\t" \
            "v_max3_u32 %[ck], %[x0], %[x1], %[x2]\n\t" \
            "v_mfma_f32_16x16x32_bf16 v[212:215], %[a03], %[m1], v[212:215]\n\t" \
            "ds_write_b128 %[l1], %[m1]\n\t" \
            "v_mfma_f32_16x16x32_bf16 v[216:219], %[a10], %[m0], 0\n\t" \
            "v_max3_u32 %[ck], %[ck], %[x3], %[x4]\n\t" \
            "v_mfma_f32_16x16x32_bf16 v[220:223], %[a11], %[m0], 0\n\t" \
            "v_max3_u32 %[ck], %[ck], %[x5], %[x6]\n\t" \
            "v_mfma_f32_16x16x32_bf16 v[216:219], %[a12], %[m1], v[216:219]\n\t" \
            "v_max_u32 %[ck], %[ck], %[x7]\n\t" \
            "v_mfma_f32_16x16x32_bf16 v[220:223], %[a13], %[m1], v[220:223]\n\t" \
            "v_mfma_f32_16x16x32_bf16 %[e2], %[a20], %[m0], 0\n\t" \
            "v_cndmask_b32 v224, v208, v212, %[pm]\n\tv_cndmask_b32 v225, v209, v213, %[pm]\n\t" \
            "v_mfma_f32_16x16x32_bf16 %[o2], %[a21], %[m0], 0\n\t" \
            "v_cvt_pk_bf16_f32 %[b0], v224, v225\n\tv_cndmask_b32 v224, v210, v214, %[pm]\n\t" \
            "v_mfma_f32_16x16x32_bf16 %[e2], %[a22], %[m1], %[e2]\n\t" \
            "v_cndmask_b32 v225, v211, v215, %[pm]\n\tv_cvt_pk_bf16_f32 %[b1], v224, v225\n\t" \
            "v_mfma_f32_16x16x32_bf16 %[o2], %[a23], %[m1], %[o2]\n\t" \
            "v_mfma_f32_16x16x32_bf16 %[e3], %[a30], %[m0], 0\n\t" \
            "v_cndmask_b32 v224, v216, v220, %[pm]\n\tv_cndmask_b32 v225, v217, v221, %[pm]\n\t" \
            "v_mfma_f32_16x16x32_bf16 %[o3], %[a31], %[m0], 0\n\t" \
            "v_cvt_pk_bf16_f32 %[b2], v224, v225\n\tv_cndmask_b32 v224, v218, v222, %[pm]\n\t" \
            "v_mfma_f32_16x16x32_bf16 %[e3], %[a32], %[m1], %[e3]\n\t" \
            "v_cndmask_b32 v225, v219, v223, %[pm]\n\tv_cvt_pk_bf16_f32 %[b3], v224, v225\n\t" \
            "v_mfma_f32_16x16x32_bf16 %[o3], %[a33], %[m1], %[o3]" \
            : [e2] "=&v"(te2), [o2] "=&v"(to2), [e3] "=&v"(te3), [o3] "=&v"(to3), [b0] "=&v"(B0.x), [b1] "=&v"(B0.y), [b2] "=&v"(B0.z), [b3] "=&v"(B0.w), [ck] "=&v"(chk) \
            : [a00] "a"(AT[0][0]), [a01] "a"(AT[0][1]), [a02] "a"(AT[0][2]), [a03] "a"(AT[0][3]), [a10] "a"(AT[1][0]), [a11] "a"(AT[1][1]), [a12] "a"(AT[1][2]), [a13] "a"(AT[1][3]), \
              [a20] "a"(AT[2][0]), [a21] "a"(AT[2][1]), [a22] "a"(AT[2][2]), [a23] "a"(AT[2][3]), [a30] "a"(AT[3][0]), [a31] "a"(AT[3][1]), [a32] "a"(AT[3][2]), [a33] "a"(AT[3][3]), \
              [m0] "v"(m0), [m1] "v"(m1), [pm] "s"(parm), [l0] "v"(ldm0a), [l1] "v"(ldm1a), \
              [x0] "v"(m0.x), [x1] "v"(m0.y), [x2] "v"(m0.z), [x3] "v"(m0.w), [x4] "v"(m1.x), [x5] "v"(m1.y), [x6] "v"(m1.z), [x7] "v"(m1.w) \
            : "memory", "v208", "v209", "v210", "v211", "v212", "v213", "v214", "v215", "v216", "v217", "v218", "v219", "v220", "v221", "v222", "v223", "v224", "v225")
        W_HEAD();
        COUNT(8, 1);
        if (__builtin_expect(__any(chk == SENT), 0)) {
            int left = __builtin_amdgcn_readfirstlane(cap);
            asm volatile(
                "buffer_load_dwordx4 v[196:199], %[a0], %[r], %[s] offen sc1\n\tbuffer_load_dwordx4 v[200:203], %[a1], %[r], %[s] offen sc1\n\t"
                "s_sleep 1\n\ts_nop 7\n"
                ".Lpm%=:\n\t"
                "buffer_load_dwordx4 v[208:211], %[a0], %[r], %[s] offen sc1\n\tbuffer_load_dwordx4 v[212:215], %[a1], %[r], %[s] offen sc1\n\t"
                "s_waitcnt vmcnt(2)\n\t"
                "v_max3_u32 v224, v196, v197, v198\n\tv_max3_u32 v224, v224, v199, v200\n\tv_max3_u32 v224, v224, v201, v202\n\tv_max_u32 v224, v224, v203\n\t"
                "v_cmp_eq_u32 vcc, -1, v224\n\ts_cbranch_vccz .Lpa%=\n\t"
                "buffer_load_dwordx4 v[196:199], %[a0], %[r], %[s] offen sc1\n\tbuffer_load_dwordx4 v[200:203], %[a1], %[r], %[s] offen sc1\n\t"
                "s_waitcnt vmcnt(2)\n\t"
                "v_max3_u32 v224, v208, v209, v210\n\tv_max3_u32 v224, v224, v211, v212\n\tv_max3_u32 v224, v224, v213, v214\n\tv_max_u32 v224, v224, v215\n\t"
                "v_cmp_eq_u32 vcc, -1, v224\n\ts_cbranch_vccz .Lpb%=\n\t"
                "s_add_i32 %[n], %[n], -1\n\ts_cmp_gt_i32 %[n], 0\n\ts_cbranch_scc1 .Lpm%=\n\t"
                "s_branch .Lpa%=\n"
                ".Lpb%=:\n\t"
                "s_waitcnt vmcnt(0)\n\t"
                "v_mov_b32 v196, v208\n\tv_mov_b32 v197, v209\n\tv_mov_b32 v198, v210\n\tv_mov_b32 v199, v211\n\tv_mov_b32 v200, v212\n\tv_mov_b32 v201, v213\n\tv_mov_b32 v202, v214\n\tv_mov_b32 v203, v215\n"
                ".Lpa%=:\n\t"
                "s_waitcnt vmcnt(0)\n\ts_nop 1"
                : "={v[196:199]}"(m0), "={v[200:203]}"(m1), [n] "+s"(left)
                : [a0] "v"(vm0), [a1] "v"(vm1), [r] "s"(rs), [s] "s"(so + MCOPY - mc0)
                : "memory", "vcc", "scc", "v208", "v209", "v210", "v211", "v212", "v213", "v214", "v215", "v224");
            COUNT(8, 1);
            if (__builtin_amdgcn_readfirstlane(left) <= 0) { note_timeout(rs, 0x100 + s); cap = 0; }
            W_HEAD();
        }
        STAMP(1, ta);
        lds_barrier();
        if (s == NSEG - 1) break;
        unsigned t2;
        {
            asm volatile(
                "ds_read_b128 v[232:235], %[a0]\n\tds_read_b128 v[236:239], %[a1]\n\t"
                "v_cndmask_b32 v224, %[e20], %[o20], %[pm]\n\tv_cndmask_b32 v225, %[e21], %[o21], %[pm]\n\tv_cvt_pk_bf16_f32 v228, v224, v225\n\t"
                "v_cndmask_b32 v224, %[e22], %[o22], %[pm]\n\tv_cndmask_b32 v225, %[e23], %[o23], %[pm]\n\tv_cvt_pk_bf16_f32 v229, v224, v225\n\t"
                "v_cndmask_b32 v224, %[e30], %[o30], %[pm]\n\tv_cndmask_b32 v225, %[e31], %[o31], %[pm]\n\tv_cvt_pk_bf16_f32 v230, v224, v225\n\t"
                "v_cndmask_b32 v224, %[e32], %[o32], %[pm]\n\tv_cndmask_b32 v225, %[e33], %[o33], %[pm]\n\tv_cvt_pk_bf16_f32 v231, v224, v225\n\t"
                "s_waitcnt lgkmcnt(0)\n\t"
                "v_mfma_f32_16x16x32_bf16 v[240:243], v[232:235], %[b0], 0\n\t"
                "v_mfma_f32_16x16x32_bf16 v[240:243], v[236:239], v[228:231], v[240:243]\n\t"
                "s_nop 7\n\t"
                "v_add_f32_dpp v240, v240, v240 row_ror:8 row_mask:0xf bank_mask:0xf\n\tv_add_f32_dpp v241, v241, v241 row_ror:8 row_mask:0xf bank_mask:0xf\n\t"
                "v_add_f32_dpp v242, v242, v242 row_ror:8 row_mask:0xf bank_mask:0xf\n\tv_add_f32_dpp v243, v243, v243 row_ror:8 row_mask:0xf bank_mask:0xf\n\t"
                "v_permlane32_swap_b32 v240, v241\n\ts_nop 0\n\tv_permlane32_swap_b32 v242, v243\n\t"
                "v_add_f32 v240, v240, v241\n\tv_add_f32 v242, v242, v243\n\tv_cndmask_b32 v240, v240, v242, %[pm]\n\t"
                "ds_write_b32 %[dst], v240\n\t"
                "s_waitcnt lgkmcnt(0)\n\ts_barrier"
                :: [a0] "v"(a0a), [a1] "v"(a1a), [b0] "v"(B0), [pm] "s"(parm), [dst] "v"(lds_sa),
                   [e20] "v"(te2[0]), [e21] "v"(te2[1]), [e22] "v"(te2[2]), [e23] "v"(te2[3]), [o20] "v"(to2[0]), [o21] "v"(to2[1]), [o22] "v"(to2[2]), [o23] "v"(to2[3]),
                   [e30] "v"(te3[0]), [e31] "v"(te3[1]), [e32] "v"(te3[2]), [e33] "v"(te3[3]), [o30] "v"(to3[0]), [o31] "v"(to3[1]), [o32] "v"(to3[2]), [o33] "v"(to3[3])
                : "memory", "v224", "v225", "v228", "v229", "v230", "v231", "v232", "v233", "v234", "v235", "v236", "v237", "v238", "v239", "v240", "v241", "v242", "v243");
#define PUBLISH(lsum, o1, o2, o3, voff, soff) asm volatile( \
                "ds_read_b32 v224, %[a]\n\tds_read_b32 v225, %[a] offset:" #o1 "\n\tds_read_b32 v226, %[a] offset:" #o2 "\n\tds_read_b32 v227, %[a] offset:" #o3 "\n\t" \
                "s_waitcnt lgkmcnt(2)\n\tv_add_f32 v224, v224, v225\n\ts_waitcnt lgkmcnt(0)\n\tv_add_f32 v226, v226, v227\n\tv_add_f32 v224, v224, v226\n\ts_nop 1\n\t" \
                "v_mov_b32_dpp v225, v224 quad_perm:[0,0,2,2] row_mask:0xf bank_mask:0xf\n\tv_mov_b32_dpp v226, v224 quad_perm:[1,1,3,3] row_mask:0xf bank_mask:0xf\n\t" \
                "v_cvt_pk_bf16_f32 v224, v225, v226\n\tv_min_u32 v224, -2, v224\n\tbuffer_store_dword v224, %[v], %[r], %[s] offen sc1" \
                :: [a] "v"(lsum), [v] "v"(voff), [r] "s"(rs), [s] "s"(soff) : "memory", "v224", "v225", "v226", "v227")
            if (w == 0) PUBLISH(lsum_s, 256, 512, 768, vspub, so);
            t2 = ct() - (w == 0 ? 150 : 0);
            STAMP(2, ta);
        }
        SCHED();
        if (__builtin_expect(__any(flg == 0), 0)) {
            int att = 0; unsigned f2;
#pragma unroll 1
            do {
                asm volatile("buffer_load_dword %0, %1, %2, %3 offen sc1\n\ts_waitcnt vmcnt(0)" : "=&v"(f2) : "v"(vflag), "s"(rs), "s"(s * 1024) : "memory");
            } while (__any(f2 == 0) && ++att < cap);
            if (att >= cap) { note_timeout(rs, 0x200 + s); cap = 0; }
        }
        u32x4 U0, U1;
        VLD16(U0, vu0, sU); VLD16(U1, vu1, sU); VLD4(flg, vflag, (s + 1) * 1024);
        f32x4 ye0, yo0, ye1, yo1;
#pragma unroll
        for (int i = 0; i < 7; ++i) mf[i] = *(const LAS u32x4*)(L + L_M + i * 1024 + l * 16);
#define Y_STEP0(i) asm volatile("v_mfma_f32_16x16x32_bf16 %0, %4, %5, 0\n\tv_mfma_f32_16x16x32_bf16 %1, %4, %6, 0\n\tv_mfma_f32_16x16x32_bf16 %2, %4, %7, 0\n\tv_mfma_f32_16x16x32_bf16 %3, %4, %8, 0" \
            : "=&v"(ye0), "=&v"(yo0), "=&v"(ye1), "=&v"(yo1) : "v"(mf[i]), "a"(BT[0][2 * i]), "a"(BT[0][2 * i + 1]), "a"(BT[1][2 * i]), "a"(BT[1][2 * i + 1]))
#define Y_STEP(i) asm volatile("v_mfma_f32_16x16x32_bf16 %0, %4, %5, %0\n\tv_mfma_f32_16x16x32_bf16 %1, %4, %6, %1\n\tv_mfma_f32_16x16x32_bf16 %2, %4, %7, %2\n\tv_mfma_f32_16x16x32_bf16 %3, %4, %8, %3" \
            : "+v"(ye0), "+v"(yo0), "+v"(ye1), "+v"(yo1) : "v"(mf[i]), "a"(BT[0][2 * i]), "a"(BT[0][2 * i + 1]), "a"(BT[1][2 * i]), "a"(BT[1][2 * i + 1]))
        Y_STEP0(0); Y_STEP(1); Y_STEP(2); Y_STEP(3); Y_STEP(4);
        SCHED();
        u32x4 s0, s1, s2;
        STAMP(3, ta);
        wait_until(t2 + tauS);
        STAMP(4, ta);
        VLD16(s0, vs0, so + sc0); VLD16(s1, vs1, so + sc0); VLD16(s2, vs2, so + sc0);
        SCHED();
        Y_STEP(5); Y_STEP(6);
#define Y_FINISH() do {   \
            asm volatile("s_nop 7" : "+v"(ye0), "+v"(yo0), "+v"(ye1), "+v"(yo1)); \
            float y[2][4]; \
            _Pragma("unroll") for (int r = 0; r < 4; ++r) { \
                const auto x = __builtin_amdgcn_permlane32_swap(__float_as_uint(yo0[r]), __float_as_uint(yo0[r]), false, false); y[0][r] = ye0[r] + __uint_as_float(x[1]); \
                const auto z = __builtin_amdgcn_permlane32_swap(__float_as_uint(yo1[r]), __float_as_uint(yo1[r]), false, false); y[1][r] = ye1[r] + __uint_as_float(z[1]); \
            } \
            YA0 = u32x2{pk2(y[0][0], y[0][1]), pk2(y[0][2], y[0][3])}; YA1 = u32x2{pk2(y[1][0], y[1][1]), pk2(y[1][2], y[1][3])}; \
            asm volatile("" : "+v"(YA0), "+v"(YA1)); } while (0)
        Y_FINISH();
        u32x2 YM0 = l < 32 ? YA0 : u32x2{0, 0}, YM1 = l < 32 ? YA1 : u32x2{0, 0};
        int vmp = toring ? vmring + ((s + 1) & 3) * SLOT : (tomx ? vmx + ((s + 1) & 3) * SLOT : vmglob + (s + 1) * 16384);
        asm volatile("" : "+v"(YM0), "+v"(YM1), "+v"(vmp));
        SCHED();
        STAMP(5, ta);
        VWAIT();
        f32x4 dxa, dxb, ds;
#define B_HEAD() asm volatile( \
            "v_mfma_f32_16x16x32_bf16 %0, %4, %6, %10\n\tv_mfma_f32_16x16x32_bf16 %0, %5, %7, %0\n\t" \
            "v_max3_u32 %3, %12, %13, %14\n\tv_max3_u32 %3, %3, %15, %16\n\t" \
            "v_mfma_f32_16x16x32_bf16 %1, %8, %11, 0\n\t" \
            "v_max3_u32 %3, %3, %17, %18\n\tv_max3_u32 %3, %3, %19, %20\n\t" \
            "v_mfma_f32_16x16x32_bf16 %2, %9, %11, 0\n\t" \
            "v_max3_u32 %3, %3, %21, %22\n\tv_max_u32 %3, %3, %23" \
            : "=&v"(ds), "=&v"(dxa), "=&v"(dxb), "=&v"(chk) \
            : "v"(SEL0), "v"(SEL1), "v"(s0), "v"(s1), "v"(U0), "v"(U1), "v"(CINIT), "v"(s2), \
              "v"(s0.x), "v"(s0.y), "v"(s0.z), "v"(s0.w), "v"(s1.x), "v"(s1.y), "v"(s1.z), "v"(s1.w), "v"(s2.x), "v"(s2.y), "v"(s2.z), "v"(s2.w))
        B_HEAD();
        COUNT(9, 1);
        if (__builtin_expect(__any(chk == SENT), 0)) {
            int left = __builtin_amdgcn_readfirstlane(cap);
            asm volatile(
                "buffer_load_dwordx4 v[184:187], %[a0], %[r], %[s] offen sc1\n\tbuffer_load_dwordx4 v[188:191], %[a1], %[r], %[s] offen sc1\n\tbuffer_load_dwordx4 v[192:195], %[a2], %[r], %[s] offen sc1\n\t"
                "s_sleep 1\n\ts_nop 7\n"
                ".Lpm%=:\n\t"
                "buffer_load_dwordx4 v[208:211], %[a0], %[r], %[s] offen sc1\n\tbuffer_load_dwordx4 v[212:215], %[a1], %[r], %[s] offen sc1\n\tbuffer_load_dwordx4 v[216:219], %[a2], %[r], %[s] offen sc1\n\t"
                "s_waitcnt vmcnt(3)\n\t"
                "v_max3_u32 v224, v184, v185, v186\n\tv_max3_u32 v224, v224, v187, v188\n\tv_max3_u32 v224, v224, v189, v190\n\tv_max3_u32 v224, v224, v191, v192\n\tv_max3_u32 v224, v224, v193, v194\n\tv_max_u32 v224, v224, v195\n\t"
                "v_cmp_eq_u32 vcc, -1, v224\n\ts_cbranch_vccz .Lpa%=\n\t"
                "buffer_load_dwordx4 v[184:187], %[a0], %[r], %[s] offen sc1\n\tbuffer_load_dwordx4 v[188:191], %[a1], %[r], %[s] offen sc1\n\tbuffer_load_dwordx4 v[192:195], %[a2], %[r], %[s] offen sc1\n\t"
                "s_waitcnt vmcnt(3)\n\t"
                "v_max3_u32 v224, v208, v209, v210\n\tv_max3_u32 v224, v224, v211, v212\n\tv_max3_u32 v224, v224, v213, v214\n\tv_max3_u32 v224, v224, v215, v216\n\tv_max3_u32 v224, v224, v217, v218\n\tv_max_u32 v224, v224, v219\n\t"
                "v_cmp_eq_u32 vcc, -1, v224\n\ts_cbranch_vccz .Lpb%=\n\t"
                "s_add_i32 %[n], %[n], -1\n\ts_cmp_gt_i32 %[n], 0\n\ts_cbranch_scc1 .Lpm%=\n\t"
                "s_branch .Lpa%=\n"
                ".Lpb%=:\n\t"
                "s_waitcnt vmcnt(0)\n\t"
                "v_mov_b32 v184, v208\n\tv_mov_b32 v185, v209\n\tv_mov_b32 v186, v210\n\tv_mov_b32 v187, v211\n\tv_mov_b32 v188, v212\n\tv_mov_b32 v189, v213\n\t"
                "v_mov_b32 v190, v214\n\tv_mov_b32 v191, v215\n\tv_mov_b32 v192, v216\n\tv_mov_b32 v193, v217\n\tv_mov_b32 v194, v218\n\tv_mov_b32 v195, v219\n"
                ".Lpa%=:\n\t"
                "s_waitcnt vmcnt(0)\n\ts_nop 1"
                : "={v[184:187]}"(s0), "={v[188:191]}"(s1), "={v[192:195]}"(s2), [n] "+s"(left)
                : [a0] "v"(vs0), [a1] "v"(vs1), [a2] "v"(vs2), [r] "s"(rs), [s] "s"(so + SCOPY - sc0)
                : "memory", "vcc", "scc", "v208", "v209", "v210", "v211", "v212", "v213", "v214", "v215", "v216", "v217", "v218", "v219", "v224");
            COUNT(9, 1);
            if (__builtin_amdgcn_readfirstlane(left) <= 0) { note_timeout(rs, 0x300 + s); cap = 0; }
            B_HEAD();
        }
        STAMP(6, ta);
        asm volatile(
            "v_max3_f32 v224, %[d0], %[d1], %[d2]\n\t"
            "v_max_f32 v224, v224, %[d3]\n\t"
            "v_mov_b32 v225, v224\n\t"
            "s_nop 1\n\t"
            "v_permlane32_swap_b32 v224, v225\n\t"
            "v_max_f32 v224, v224, v225\n\t"
            "v_mov_b32 v225, v224\n\t"
            "s_nop 1\n\t"
            "v_permlane16_swap_b32 v224, v225\n\t"
            "v_max_f32 v224, v224, v225\n\t"
            "v_mul_f32 v224, 0xbdb8aa3b, v224\n\t"
            "v_fmamk_f32 v226, %[d0], 0x3db8aa3b, v224\n\t"
            "v_fmamk_f32 v227, %[d1], 0x3db8aa3b, v224\n\t"
            "v_exp_f32 v226, v226\n\t"
            "v_fmamk_f32 v228, %[d2], 0x3db8aa3b, v224\n\t"
            "v_exp_f32 v227, v227\n\t"
            "v_fmamk_f32 v229, %[d3], 0x3db8aa3b, v224\n\t"
            "v_exp_f32 v228, v228\n\t"
            "v_exp_f32 v229, v229\n\t"
            "v_cvt_pk_bf16_f32 v230, v226, v227\n\t"
            "v_cvt_pk_bf16_f32 v231, v228, v229\n\t"
            "v_cndmask_b32 v224, %[xa0], %[xb0], %[hb]\n\tv_cndmask_b32 v225, %[xa1], %[xb1], %[hb]\n\t"
            "v_mfma_f32_16x16x16_bf16 v[232:235], %[one], v[230:231], 0\n\t"
            "v_mfma_f32_16x16x16_bf16 v[236:239], %[aw], v[230:231], 0\n\t"
            "v_mfma_f32_16x16x16_bf16 v[240:243], %[y0], v[230:231], 0\n\t"
            "v_mfma_f32_16x16x16_bf16 v[244:247], %[y1], v[230:231], 0\n\t"
            "v_cndmask_b32 v226, %[xa2], %[xb2], %[hb]\n\tv_cndmask_b32 v227, %[xa3], %[xb3], %[hb]\n\t"
            "s_nop 2\n\t"
            "v_rcp_f32 v228, v232\n\t"
            "s_nop 0\n\t"
            "v_cndmask_b32 v228, 0, v228, %[hv]\n\t"
            "v_mul_f32 v229, v236, v228\n\t"
            "v_cndmask_b32 v240, v240, v244, %[hb]\n\tv_cndmask_b32 v241, v241, v245, %[hb]\n\t"
            "v_cndmask_b32 v242, v242, v246, %[hb]\n\tv_cndmask_b32 v243, v243, v247, %[hb]\n\t"
            "v_pk_mul_f32 v[240:241], v[240:241], v[228:229] op_sel_hi:[1,0]\n\tv_pk_mul_f32 v[242:243], v[242:243], v[228:229] op_sel_hi:[1,0]\n\t"
            "v_pk_fma_f32 v[240:241], v[224:225], v[228:229], v[240:241] op_sel:[0,1,0] op_sel_hi:[1,1,1]\n\t"
            "v_pk_fma_f32 v[242:243], v[226:227], v[228:229], v[242:243] op_sel:[0,1,0] op_sel_hi:[1,1,1]\n\t"
            "s_nop 0\n\t"
            "v_add_f32_dpp v240, v240, v240 row_ror:4 row_mask:0xf bank_mask:0xf\n\tv_add_f32_dpp v241, v241, v241 row_ror:4 row_mask:0xf bank_mask:0xf\n\t"
            "v_add_f32_dpp v242, v242, v242 row_ror:4 row_mask:0xf bank_mask:0xf\n\tv_add_f32_dpp v243, v243, v243 row_ror:4 row_mask:0xf bank_mask:0xf\n\t"
            "v_add_f32_dpp v240, v240, v240 row_ror:8 row_mask:0xf bank_mask:0xf\n\tv_add_f32_dpp v241, v241, v241 row_ror:8 row_mask:0xf bank_mask:0xf\n\t"
            "v_add_f32_dpp v242, v242, v242 row_ror:8 row_mask:0xf bank_mask:0xf\n\tv_add_f32_dpp v243, v243, v243 row_ror:8 row_mask:0xf bank_mask:0xf\n\t"
            "v_permlane32_swap_b32 v240, v241\n\ts_nop 0\n\tv_permlane32_swap_b32 v242, v243\n\t"
            "v_add_f32 v240, v240, v241\n\tv_add_f32 v242, v242, v243\n\tv_cndmask_b32 v240, v240, v242, %[h1]\n\t"
            "ds_write_b32 %[dst], v240\n\t"
            "s_waitcnt lgkmcnt(0)\n\ts_barrier"
            :: [d0] "v"(ds[0]), [d1] "v"(ds[1]), [d2] "v"(ds[2]), [d3] "v"(ds[3]), [one] "v"(AONE), [aw] "v"(AW), [y0] "v"(YM0), [y1] "v"(YM1),
               [hb] "s"(hbm), [hv] "s"(hvm), [h1] "s"(h1m), [dst] "v"(lds_oa),
               [xa0] "v"(dxa[0]), [xa1] "v"(dxa[1]), [xa2] "v"(dxa[2]), [xa3] "v"(dxa[3]), [xb0] "v"(dxb[0]), [xb1] "v"(dxb[1]), [xb2] "v"(dxb[2]), [xb3] "v"(dxb[3])
            : "memory", "v224", "v225", "v226", "v227", "v228", "v229", "v230", "v231", "v232", "v233", "v234", "v235", "v236", "v237", "v238", "v239",
              "v240", "v241", "v242", "v243", "v244", "v245", "v246", "v247");
        if (w == 0) PUBLISH(lsum_o, 128, 256, 384, vmp, 0);
        tref = ct() - (w == 0 ? 150 : 0);
        STAMP(7, ta);
        SCHED();
        tail();
        SCHED();
        stores(s);
    }
    {
        f32x4 ye0, yo0, ye1, yo1;
#pragma unroll
        for (int i = 0; i < 7; ++i) mf[i] = *(const LAS u32x4*)(L + L_M + i * 1024 + l * 16);
        Y_STEP0(0); Y_STEP(1); Y_STEP(2); Y_STEP(3); Y_STEP(4); Y_STEP(5); Y_STEP(6);
        Y_FINISH();
    }
    tail();
    stores(NSEG - 1);
    if (w == 0) { const unsigned z = 0; VST4(z, (toring || tomx) ? OOB : vmglob, NSEG * 16384); }
    DRAIN();
    lds_barrier();
    if (w == 0) { const unsigned z = 0; VST4(z, (toring || tomx) ? OOB : vmglob, (NSEG + 1) * 16384); }
    DRAIN();
}

DI void chain_mx(const Params& p, int h, LAS unsigned char* L) {
    const int tid = threadIdx.x, l = tid & 63, w = __builtin_amdgcn_readfirstlane(tid >> 6), q = l >> 4;
    const int parl = (l >> 3) & 1;
    const rsrc_t rs = __builtin_amdgcn_make_buffer_rsrc(p.ws, 0, (int)WS_END, 0x00020000);
    const int ring = (int)OFF_RING + ring_parity(p) * 8192;
    const int lo_m = (4 * parl + q) * 128 + (l & 7) * 16;
    const int vm0 = ring + MXOFF + min(2 * w, 6) * 1024 + lo_m, vm1 = w < 3 ? ring + MXOFF + (2 * w + 1) * 1024 + lo_m : OOB;
    int vz[2][4];
#pragma unroll
    for (int kt = 0; kt < 2; ++kt)
#pragma unroll
        for (int kk = 0; kk < 4; ++kk) vz[kt][kk] = (int)OFF_Z + h * 28 * 1024 + l * 16 + (kt * 14 + min(4 * w + kk, 13)) * 1024;
    const int vflag = (int)OFF_FLAG_ZU + (h * 7 + min(l & 7, 6)) * 4;
    const int tok = 2 * w + (l >> 5), key = l & 31;
    const int vppub = ring + SOFF + (l & 1) * SCOPY + s_piece(h, 8 + (key >> 3), tok) + ((key & 7) >> 1) * 4;
    const int vlpub = key < 8 ? ring + SOFF + ((key >> 2) & 1) * SCOPY + s_piece(h, 7, tok) + (key & 3) * 4 : OOB;
    const unsigned long long parm = __builtin_amdgcn_ballot_w64(parl != 0), k0m = __builtin_amdgcn_ballot_w64((key & 3) == 0), k1m = __builtin_amdgcn_ballot_w64((key & 3) == 1);
    LAS unsigned char* const lds_w = L + L_X + w * 1024 + ((l & 7) * 32 + 16 * parl + 4 * q) * 4;
    const LAS float* const xp = (const LAS float*)(L + L_X) + tok * 32 + key;
    const unsigned SENTV = SENT;
    int tau = 0;
    unsigned slot = 0;
#define XSTAMP(i)
    int natt = 0, extra = 0;
    unsigned xs_z = 0, xs_f = 0;
    u32x4 Z00, Z01, Z02, Z03, Z10, Z11, Z12, Z13;
#define LOAD_Z(sz) do { VLD16(Z00, vz[0][0], sz); VLD16(Z01, vz[0][1], sz); VLD16(Z02, vz[0][2], sz); VLD16(Z03, vz[0][3], sz); \
                        VLD16(Z10, vz[1][0], sz); VLD16(Z11, vz[1][1], sz); VLD16(Z12, vz[1][2], sz); VLD16(Z13, vz[1][3], sz); } while (0)
#define WAIT_FLAG(sg) _Pragma("unroll 1") for (int att = 0;; ++att) { unsigned f2; asm volatile("buffer_load_dword %0, %1, %2, %3 offen sc1\n\ts_waitcnt vmcnt(0)" : "=&v"(f2) : "v"(vflag), "s"(rs), "s"((sg) * 1024) : "memory"); \
        if (!__any(f2 == 0)) break; if (att > SPIN_CAP) { note_timeout(rs, 0x400 + (sg)); return; } __builtin_amdgcn_s_sleep(2); }
    unsigned flg;
    WAIT_FLAG(0);
    LOAD_Z(0);
    VLD4(flg, vflag, 1024);
    tau = p.tauXm;
    unsigned tref = ct();
    for (int s = 0; s < NSEG - 1; ++s) {
        const int so = (s & 3) * SLOT;
        if (s > 0) wait_until(tref + tau + extra);
        XSTAMP(0);
        u32x4 m0, m1;
        VLD16(m0, vm0, so); VLD16(m1, vm1, so);
        VWAIT();
        f32x4 te0, to0, te1, to1; unsigned chk;
#define X_HEAD() asm volatile( \
            "v_mfma_f32_16x16x32_bf16 %0, %5, %13, 0\n\tv_mfma_f32_16x16x32_bf16 %1, %6, %13, 0\n\t" \
            "v_max3_u32 %4, %15, %16, %17\n\t" \
            "v_mfma_f32_16x16x32_bf16 %2, %9, %13, 0\n\t" \
            "v_max3_u32 %4, %4, %18, %19\n\t" \
            "v_mfma_f32_16x16x32_bf16 %3, %10, %13, 0\n\t" \
            "v_max3_u32 %4, %4, %20, %21\n\t" \
            "v_mfma_f32_16x16x32_bf16 %0, %7, %14, %0\n\t" \
            "v_max_u32 %4, %4, %22\n\t" \
            "v_mfma_f32_16x16x32_bf16 %1, %8, %14, %1\n\tv_mfma_f32_16x16x32_bf16 %2, %11, %14, %2\n\tv_mfma_f32_16x16x32_bf16 %3, %12, %14, %3" \
            : "=&v"(te0), "=&v"(to0), "=&v"(te1), "=&v"(to1), "=&v"(chk) \
            : "v"(Z00), "v"(Z01), "v"(Z02), "v"(Z03), "v"(Z10), "v"(Z11), "v"(Z12), "v"(Z13), "v"(m0), "v"(m1), \
              "v"(m0.x), "v"(m0.y), "v"(m0.z), "v"(m0.w), "v"(m1.x), "v"(m1.y), "v"(m1.z), "v"(m1.w))
        X_HEAD();
        int att = 0;
        if (__any(chk == SENT)) {
            __builtin_amdgcn_s_sleep(RDELAY);
#pragma unroll 1
            for (att = 1;; ++att) {
                asm volatile("buffer_load_dwordx4 %0, %2, %4, %5 offen sc1\n\tbuffer_load_dwordx4 %1, %3, %4, %5 offen sc1\n\ts_waitcnt vmcnt(0)" : "=&v"(m0), "=&v"(m1) : "v"(vm0), "v"(vm1), "s"(rs), "s"(so) : "memory");
                if (!bad2(m0, m1)) break;
                if (att > SPIN_CAP) { note_timeout(rs, 0x500 + s); return; }
            }
            X_HEAD();
            asm volatile("s_nop 1");
        }
        XSTAMP(1);
        f32x4 v;
        {
            float a0, a1, a2, a3, b0, b1, b2, b3;
            asm volatile(
                "s_nop 5\n\t"
                "v_cndmask_b32 %4, %[e10], %[o00], %[pm]\n\tv_cndmask_b32 %5, %[e11], %[o01], %[pm]\n\tv_cndmask_b32 %6, %[e12], %[o02], %[pm]\n\tv_cndmask_b32 %7, %[e13], %[o03], %[pm]\n\t"
                "v_cndmask_b32 %0, %[e00], %[o10], %[pm]\n\tv_cndmask_b32 %1, %[e01], %[o11], %[pm]\n\tv_cndmask_b32 %2, %[e02], %[o12], %[pm]\n\tv_cndmask_b32 %3, %[e03], %[o13], %[pm]\n\t"
                "v_add_f32_dpp %0, %4, %0 row_ror:8 row_mask:0xf bank_mask:0xf\n\tv_add_f32_dpp %1, %5, %1 row_ror:8 row_mask:0xf bank_mask:0xf\n\t"
                "v_add_f32_dpp %2, %6, %2 row_ror:8 row_mask:0xf bank_mask:0xf\n\tv_add_f32_dpp %3, %7, %3 row_ror:8 row_mask:0xf bank_mask:0xf"
                : "=&v"(a0), "=&v"(a1), "=&v"(a2), "=&v"(a3), "=&v"(b0), "=&v"(b1), "=&v"(b2), "=&v"(b3)
                : [pm] "s"(parm),
                  [e00] "v"(te0[0]), [e01] "v"(te0[1]), [e02] "v"(te0[2]), [e03] "v"(te0[3]), [o00] "v"(to0[0]), [o01] "v"(to0[1]), [o02] "v"(to0[2]), [o03] "v"(to0[3]),
                  [e10] "v"(te1[0]), [e11] "v"(te1[1]), [e12] "v"(te1[2]), [e13] "v"(te1[3]), [o10] "v"(to1[0]), [o11] "v"(to1[1]), [o12] "v"(to1[2]), [o13] "v"(to1[3]));
            v = f32x4{a0, a1, a2, a3};
        }
        *(LAS f32x4*)lds_w = v;
        lds_barrier();
        {
            const float sv = ((xp[0] + xp[256]) + (xp[512] + xp[768])) * SC2;
            float mx, t, e, sum, pn, lg;
            asm volatile(
                "s_nop 1\n\t"
                "v_max_f32_dpp %0, %4, %4 row_ror:1 row_mask:0xf bank_mask:0xf\n\ts_nop 1\n\t"
                "v_max_f32_dpp %0, %0, %0 row_ror:2 row_mask:0xf bank_mask:0xf\n\ts_nop 1\n\t"
                "v_max_f32_dpp %0, %0, %0 row_ror:4 row_mask:0xf bank_mask:0xf\n\ts_nop 1\n\t"
                "v_max_f32_dpp %0, %0, %0 row_ror:8 row_mask:0xf bank_mask:0xf\n\t"
                "v_mov_b32 %1, %0\n\ts_nop 1\n\t"
                "v_permlane16_swap_b32 %0, %1\n\t"
                "v_max_f32 %0, %0, %1\n\t"
                "v_sub_f32 %2, %4, %0\n\t"
                "v_exp_f32 %2, %2\n\ts_nop 1\n\t"
                "v_add_f32_dpp %3, %2, %2 row_ror:1 row_mask:0xf bank_mask:0xf\n\ts_nop 1\n\t"
                "v_add_f32_dpp %3, %3, %3 row_ror:2 row_mask:0xf bank_mask:0xf\n\ts_nop 1\n\t"
                "v_add_f32_dpp %3, %3, %3 row_ror:4 row_mask:0xf bank_mask:0xf\n\ts_nop 1\n\t"
                "v_add_f32_dpp %3, %3, %3 row_ror:8 row_mask:0xf bank_mask:0xf\n\t"
                "v_mov_b32 %1, %3\n\ts_nop 1\n\t"
                "v_permlane16_swap_b32 %3, %1\n\t"
                "v_add_f32 %3, %3, %1"
                : "=&v"(mx), "=&v"(t), "=&v"(e), "=&v"(sum) : "v"(sv));
            pn = e * __builtin_amdgcn_rcpf(sum);
            const unsigned pv = min(pk2(dpp<QP0022>(pn), dpp<QP1133>(pn)), SENT - 1);
            VST4(pv, vppub, so);
            lg = __builtin_amdgcn_logf(sum);
            const float lse = (mx + lg) * (1.0f / SC2);
            const float hi = __uint_as_float(pk2(lse, 0.f) << 16), r1 = lse - hi, mid = __uint_as_float(pk2(r1, 0.f) << 16), lo = r1 - mid;
            const unsigned d0 = pk2(hi, mid), d1 = pk2(lo, 0.f);
            const unsigned lv = min((key & 3) == 0 ? d0 : ((key & 3) == 1 ? d1 : 0u), SENT - 1);
            VST4(lv, vlpub, so);
        }
        XSTAMP(2);
        tref = ct();
        if (s > 2) tau = min(max(tau + (att == 0 ? -XDN : XUP), p.tauXm - 400), p.tauXm + XCLAMP);
        extra = att ? 2 * EXTRA : 0;
        if (s >= 8) natt += att + 1;
        lds_barrier();
        __builtin_amdgcn_s_sleep(2);
        { const int sp = ((s - 1) & 3) * SLOT; VST4(SENTV, vppub, sp); VST4(SENTV, vlpub, sp); }
        if (__any(flg == 0)) { WAIT_FLAG(s + 1); }
        LOAD_Z((s + 1) * (16 * 28 * 1024));
        VLD4(flg, vflag, min(s + 2, NSEG - 1) * 1024);
    }
    DRAIN();
}

constexpr int XB = 32768;
DI void x_dma(const Params& p, int s, LAS unsigned char* buf, int w, int l) {
#pragma unroll
    for (int i = 0; i < 8; ++i) {
        const int r = 8 * w + i;
        int ch = l ^ (r & 15);
        ch = ch < 56 ? ch : ch - 8;
        __builtin_amdgcn_global_load_lds((const GAS unsigned*)(p.x + ((size_t)s * 32 + r) * D + ch * 8), (LAS unsigned*)(buf + r * 1024), 16, 0, 0);
    }
}
DI u32x4 swz_lo(u32x2& a, u32x2& b) {
    const auto x = __builtin_amdgcn_permlane32_swap(a.x, b.x, false, false);
    const auto y = __builtin_amdgcn_permlane32_swap(a.y, b.y, false, false);
    return u32x4{x[0], y[0], x[1], y[1]};
}
template <int NKS> DI void load_tr(u32x4 (&W)[NKS], const u16* src, int ld, LAS unsigned char* stg, int l) {
    constexpr int HALF = NKS / 2;
#pragma unroll
    for (int hf = 0; hf < 2; ++hf) {
        asm volatile("s_waitcnt lgkmcnt(0)" ::: "memory");
        for (int i = 0; i < HALF; ++i) {
            const int r = 16 * i + (l >> 2);
            *(LAS u32x4*)(stg + r * 64 + (l & 3) * 16) = gld16(src + (size_t)(hf * HALF * 16 + r) * ld + (l & 3) * 8);
        }
        asm volatile("s_waitcnt lgkmcnt(0)" ::: "memory");
#pragma unroll
        for (int k = 0; k < HALF; ++k) {
            const LAS unsigned char* a = stg + (16 * k + 8 * (l >> 5) + ((l & 15) >> 2)) * 64 + ((l >> 4) & 1) * 32 + (l & 3) * 8;
            const u32x2 x = __builtin_bit_cast(u32x2, __builtin_amdgcn_ds_read_tr16_b64_v4i16((LAS s16x4*)a));
            const u32x2 y = __builtin_bit_cast(u32x2, __builtin_amdgcn_ds_read_tr16_b64_v4i16((LAS s16x4*)(a + 4 * 64)));
            W[hf * HALF + k] = u32x4{x.x, x.y, y.x, y.y};
        }
    }
}

DI void x_dma1(const Params& p, int s, LAS unsigned char* buf, int r, int ch8) {
    __builtin_amdgcn_global_load_lds((const GAS unsigned*)(p.x + ((size_t)s * 32 + r) * D + ch8), (LAS unsigned*)(buf + r * 1024), 16, 0, 0);
}
template <int KIND>
DI void stream_x(const Params& p, int cu, LAS unsigned char* L) {
    const int tid = threadIdx.x, l = tid & 63, w = __builtin_amdgcn_readfirstlane(tid >> 6), hi = l >> 5, tk = l & 31;
    const rsrc_t rs = __builtin_amdgcn_make_buffer_rsrc(p.ws, 0, (int)WS_END, 0x00020000);
    const int wid = cu * 4 + w;
    u32x4 W[2][28];
    int h = 0, b = 0;
    x_dma(p, 0, L, w, l);
    if (KIND < 2) {
        const int T = wid * 2; h = T / 14; b = T % 14;
        const unsigned char* src = p.ws + (KIND == 0 ? OFF_A : OFF_BT);
#pragma unroll
        for (int ks = 0; ks < 28; ++ks)
#pragma unroll
            for (int t = 0; t < 2; ++t) W[t][ks] = gld16(src + piece(h, b + t, 2 * ks + hi, tk));
        SCHED();
    } else {
        LAS unsigned char* stg = L + 2 * XB + w * (14 * 16 * 64);
        load_tr<28>(W[0], p.Wg + 32 * wid, FF, stg, l);
        load_tr<28>(W[1], p.Wu + 32 * wid, FF, stg, l);
    }
    const int vflag = KIND == 2 ? (int)OFF_FLAG_H + wid * 4 : (int)OFF_FLAG_ZU + ((KIND == 1 ? 112 : 0) + wid) * 4;
    const int fstride = KIND == 2 ? 256 : 1024;
    constexpr int NST = KIND == 2 ? 2 : 4;
    const LAS unsigned char* xrow = L + tk * 1024;
    int xo[8], ch8[8];
#pragma unroll
    for (int k = 0; k < 8; ++k) { xo[k] = ((2 * k + hi) ^ (tk & 15)) * 16; int ch = l ^ ((8 * w + k) & 15); ch8[k] = (ch < 56 ? ch : ch - 8) * 8; }
    const int vst = KIND == 0 ? (int)OFF_Z + ((h * 2 + (tk >> 4)) * 14 + b) * 1024 + (hi * 16 + (tk & 15)) * 16
                  : KIND == 1 ? (int)OFF_U + ((h * 5 + hi) * D + 32 * b + tk) * 16 : (int)OFF_H + ((4 * wid + hi) * 32 + tk) * 16;
    constexpr int sstr = KIND == 0 ? 16 * 28 * 1024 : KIND == 1 ? 16 * 5 * D * 16 : 224 * 512;
    auto epi = [&](const f32x16 (&a)[2], int s, int i) __attribute__((always_inline)) {
        if (KIND == 0) {
            const int t = i >> 1, P = i & 1;
            const u32x4 v = {pk2(a[t][4 * P + 0], a[t][4 * P + 1]), pk2(a[t][4 * P + 2], a[t][4 * P + 3]), pk2(a[t][8 + 4 * P + 0], a[t][8 + 4 * P + 1]), pk2(a[t][8 + 4 * P + 2], a[t][8 + 4 * P + 3])};
            bst16(v, rs, vst + t * 1024 + P * 512, s * sstr);
        } else if (KIND == 1) {
            const int t = i >> 1, P = i & 1;
            u32x2 x = {pk2(a[t][8 * P + 0], a[t][8 * P + 1]), pk2(a[t][8 * P + 2], a[t][8 * P + 3])};
            u32x2 y = {pk2(a[t][8 * P + 4], a[t][8 * P + 5]), pk2(a[t][8 * P + 6], a[t][8 * P + 7])};
            const u32x4 v = swz_lo(x, y);
            bst16(v, rs, vst + t * 512 + P * (2 * D * 16), s * sstr);
        } else {
            float hv[8];
#pragma unroll
            for (int k = 0; k < 8; ++k) { const float gg = a[0][8 * i + k]; hv[k] = gg * __builtin_amdgcn_rcpf(1.f + __builtin_amdgcn_exp2f(-1.4426950408889634f * gg)) * a[1][8 * i + k]; }
            u32x2 x = {pk2(hv[0], hv[1]), pk2(hv[2], hv[3])}, y = {pk2(hv[4], hv[5]), pk2(hv[6], hv[7])};
            const u32x4 v = swz_lo(x, y);
            bst16(v, rs, vst + i * 1024, s * sstr);
        }
    };
    auto tile = [&](int s, f32x16 (&acc)[2], const f32x16 (&prev)[2], bool first = false) __attribute__((always_inline)) {
        if (first && KIND < 2) asm volatile("s_waitcnt vmcnt(56)" ::: "memory");
        else if (s < 4) { DRAIN(); if (s >= 2 && l == 0) bst4(1u, rs, vflag, (s - 2) * fstride); }
        else { asm volatile("s_waitcnt vmcnt(%0)" :: "n"(NST) : "memory"); if (l == 0) { bst4(1u, rs, vflag, (s - 3) * fstride); if (s == 4) bst4(1u, rs, vflag, 2 * fstride); } }
        lds_barrier();
        const LAS unsigned char* xb = xrow + (s & 1) * XB;
        LAS unsigned char* nb = L + ((s + 1) & 1) * XB;
        const int sn = min(s + 1, NSEG - 1);
        u32x4 xf[4];
#pragma unroll
        for (int k = 0; k < 3; ++k) xf[k] = *(const LAS u32x4*)(xb + xo[k]);
#pragma unroll
        for (int ks = 0; ks < 28; ++ks) {
            if (ks + 3 < 28) xf[(ks + 3) & 3] = *(const LAS u32x4*)(xb + xo[(ks + 3) & 7] + ((ks + 3) >> 3) * 256);
            SCHED();
            if (ks == 0) {
                if (KIND == 1) { mma32_wb<true>(acc[0], xf[0], W[0][0]); mma32_wb<true>(acc[1], xf[0], W[1][0]); }
                else { mma32_wa<true>(acc[0], W[0][0], xf[0]); mma32_wa<true>(acc[1], W[1][0], xf[0]); }
            } else {
                if (KIND == 1) { mma32_wb<false>(acc[0], xf[ks & 3], W[0][ks]); mma32_wb<false>(acc[1], xf[ks & 3], W[1][ks]); }
                else { mma32_wa<false>(acc[0], W[0][ks], xf[ks & 3]); mma32_wa<false>(acc[1], W[1][ks], xf[ks & 3]); }
            }
            SCHED();
            if (ks >= 1 && ks < 9) x_dma1(p, sn, nb, 8 * w + ks - 1, ch8[ks - 1]);
            if (ks >= 10 && (ks - 10) % (KIND == 2 ? 8 : 4) == 0 && (ks - 10) / (KIND == 2 ? 8 : 4) < NST && s > 1) epi(prev, s - 1, (ks - 10) / (KIND == 2 ? 8 : 4));
            SCHED();
        }
    };
    f32x16 accA[2], accB[2];
    tile(0, accB, accA, true);
    mma32_done(accB[0], accB[1]);
#pragma unroll
    for (int i = 0; i < NST; ++i) epi(accB, 0, i);
    DRAIN();
    if (l == 0) bst4(1u, rs, vflag, 0);
    tile(1, accB, accA);
#pragma unroll 1
    for (int s = 2; s < NSEG; s += 2) { tile(s, accA, accB); tile(s + 1, accB, accA); }
    mma32_done(accB[0], accB[1]);
#pragma unroll
    for (int i = 0; i < NST; ++i) epi(accB, NSEG - 1, i);
    DRAIN();
    if (l == 0) { bst4(1u, rs, vflag, (NSEG - 3) * fstride); bst4(1u, rs, vflag, (NSEG - 2) * fstride); bst4(1u, rs, vflag, (NSEG - 1) * fstride); }
}

DI void ffn_down(const Params& p, int cu14, LAS unsigned char* L) {
    const int cu = cu14 % 7, grp = cu14 / 7;
    const int tid = threadIdx.x, l = tid & 63, w = __builtin_amdgcn_readfirstlane(tid >> 6), hi = l >> 5, tk = l & 31;
    const rsrc_t rs = __builtin_amdgcn_make_buffer_rsrc(p.ws, 0, (int)WS_END, 0x00020000);
    u32x4 W[2][28];
    {
        LAS unsigned char* stg = L + w * (14 * 16 * 64);
        load_tr<28>(W[0], p.Wd + (size_t)(448 * w) * D + 64 * cu, D, stg, l);
        load_tr<28>(W[1], p.Wd + (size_t)(448 * w) * D + 64 * cu + 32, D, stg, l);
    }
    lds_barrier();
    const int vh = (int)OFF_H + (56 * w) * 512 + l * 16;
    const int vflag = (int)OFF_FLAG_H + (14 * w + min(l, 13)) * 4;
    LAS float* red = (LAS float*)L;
    float* fo = (float*)(p.ws + OFF_FO);
    auto wait_h = [&](int s) __attribute__((always_inline)) -> bool {
        for (int att = 0;; ++att) {
            asm volatile("" ::: "memory");
            const unsigned f = bld4(rs, vflag, s * 256);
            if (!__any(f == 0)) return true;
            if (att > SPIN_CAP) { note_timeout(rs, 0x600 + s); return false; }
            __builtin_amdgcn_s_sleep(8);
        }
    };
    if (!wait_h(grp)) return;
    u32x4 hf[28];
#pragma unroll
    for (int ks = 0; ks < 28; ++ks) hf[ks] = bld16(rs, vh + ks * 1024, grp * (224 * 512));
    for (int s = grp; s < NSEG; s += 2) {
        const int sn = min(s + 2, NSEG - 2 + grp);
        if (!wait_h(sn)) return;
        f32x16 acc[2];
#pragma unroll
        for (int ks = 0; ks < 28; ++ks) {
            if (ks == 0) { mma32_wb<true>(acc[0], hf[0], W[0][0]); mma32_wb<true>(acc[1], hf[0], W[1][0]); }
            else { mma32_wb<false>(acc[0], hf[ks], W[0][ks]); mma32_wb<false>(acc[1], hf[ks], W[1][ks]); }
            SCHED();
            hf[ks] = bld16(rs, vh + ks * 1024, sn * (224 * 512));
            SCHED();
        }
        mma32_done(acc[0], acc[1]);
        lds_barrier();
#pragma unroll
        for (int t = 0; t < 2; ++t)
#pragma unroll
            for (int i = 0; i < 16; ++i) red[((w * 2 + t) * 16 + i) * 64 + l] = acc[t][i];
        lds_barrier();
#pragma unroll
        for (int t = 0; t < 2; ++t)
#pragma unroll
            for (int r = 0; r < 4; ++r) {
                const int i = 4 * w + r;
                const float v = (red[((0 * 2 + t) * 16 + i) * 64 + l] + red[((1 * 2 + t) * 16 + i) * 64 + l]) + (red[((2 * 2 + t) * 16 + i) * 64 + l] + red[((3 * 2 + t) * 16 + i) * 64 + l]);
                bst4(__float_as_uint(v), rs, (int)OFF_FO + ((8 * w + 4 * hi + r) * D + 64 * cu + 32 * t + tk) * 4, s * (32 * D * 4));
            }
        DRAIN();
        if (l == 0) bst4(1u, rs, (int)OFF_FLAG_F + (cu * 4 + w) * 4, s * 128);
    }
}

template <int DATA>
DI bool wait_words(rsrc_t rs, int voff, int soff, unsigned code, bool fast) {
#pragma unroll 1
    for (int att = 0;; ++att) {
        asm volatile("" ::: "memory");
        const unsigned f = bld4(rs, voff, soff);
        if (!__any(f == (DATA ? SENT : 0u))) return true;
        if (att > SPIN_CAP) { note_timeout(rs, code); return false; }
        if (fast) __builtin_amdgcn_s_sleep(2); else __builtin_amdgcn_s_sleep(POLL_SLEEP);
    }
}
#define RST(i)
DI void rows_role(const Params& p, int id, LAS unsigned char* L) {
    const int tid = threadIdx.x, l = tid & 63, w = __builtin_amdgcn_readfirstlane(tid >> 6), r16 = l & 15, q = l >> 4;
    const int grp = id >> 2, qi = id & 3;
    const rsrc_t rs = __builtin_amdgcn_make_buffer_rsrc(p.ws, 0, (int)WS_END, 0x00020000);
    LAS f32x4* red = (LAS f32x4*)(L + XB);
    LAS float* gate = (LAS float*)(L + XB + 4 * 14 * 1024);
    const int h = 4 * qi + w;
    const int k0 = 8 * (r16 >> 2) + (r16 & 3), k1 = k0 + 4;
    const int vz0 = (int)OFF_Z + h * 28 * 1024 + (k0 >> 4) * 14 * 1024 + (q * 16 + (k0 & 15)) * 16, vz1 = (int)OFF_Z + h * 28 * 1024 + (k1 >> 4) * 14 * 1024 + (q * 16 + (k1 & 15)) * 16;
    const int vzm = (int)OFF_ZM + h * 14 * 512 + q * 128 + (r16 & 7) * 16;
    const int mall = (int)OFF_M + ring_parity(p) * 8192;
    for (int i = 0; i < PRETOUCH; ++i) __builtin_amdgcn_s_sleep(127);
#pragma unroll 1
    for (int sb = 0; sb < NSEG; sb += 16) {
        u32x4 t0[16], t1[16];
#pragma unroll
        for (int k16 = 0; k16 < 16; ++k16) {
            const int sg = sb + k16, u = (id * 4 + w + sg * 37) % 176;
            { const int hh = u / 14, k = u % 14; t0[k16] = bld16(rs, k < 7 ? (int)OFF_U + ((hh * 5 + 4) * D) * 16 + k * 1024 + l * 16 + sg * (16 * 5 * D * 16) : (int)OFF_ZM + hh * 7168 + (k - 7) * 1024 + l * 16 + sg * (16 * 14 * 512), 0); }
            { const int u2 = u + 176, hh = u2 / 14, k = u2 % 14; t1[k16] = bld16(rs, u >= 48 ? OOB : k < 7 ? (int)OFF_U + ((hh * 5 + 4) * D) * 16 + k * 1024 + l * 16 + sg * (16 * 5 * D * 16) : (int)OFF_ZM + hh * 7168 + (k - 7) * 1024 + l * 16 + sg * (16 * 14 * 512), 0); }
        }
#pragma unroll
        for (int k16 = 0; k16 < 16; ++k16) asm volatile("" :: "v"(t0[k16]), "v"(t1[k16]));
    }
    const int b0 = (112 * qi) >> 5, b1 = (112 * qi + 111) >> 5;
    const int rz = 7 * h + min(l & 7, 6);
    const int vmw = l < 8 ? mall + (rz % 56) * 128 + (rz / 56) * 64 : mall + (14 * qi + min(l - 8, 13)) * 128;
#pragma unroll 1
    for (int s = grp; s < NSEG; s += 11) {
        const int pb = grp * 2 + ((s / 11) & 1);
        const bool late = s >= NSEG - 6;
        lds_barrier();
        x_dma(p, s, L, w, l);
        if (!wait_words<0>(rs, (int)OFF_FLAG_ZU + ((l >= 8 && l < 40) ? 112 + ((4 * w + ((l - 8) >> 3)) * 14 + min(b0 + (l & 7), b1)) / 2 : 7 * h + min(l & 7, 6)) * 4, s * 1024, 0x700 + s, false)) return;
        u32x4 z0[14], z1[14], uf[4][7];
#pragma unroll
        for (int ks = 0; ks < 14; ++ks) { z0[ks] = bld16(rs, vz0 + ks * 1024, s * (16 * 28 * 1024)); z1[ks] = bld16(rs, vz1 + ks * 1024, s * (16 * 28 * 1024)); }
#pragma unroll
        for (int hh = 0; hh < 4; ++hh)
#pragma unroll
            for (int t = 0; t < 7; ++t) uf[hh][t] = bld16(rs, (int)OFF_U + (((4 * w + hh) * 5 + q) * D + 112 * qi + 16 * t + r16) * 16, s * (16 * 5 * D * 16));
        DRAIN();
        lds_barrier();
        f32x4 sc[3][2];
#pragma unroll
        for (int i = 0; i < 3; ++i) { sc[i][0] = f32x4{0, 0, 0, 0}; sc[i][1] = sc[i][0]; }
#pragma unroll
        for (int ks = 0; ks < 14; ++ks)
#pragma unroll
            for (int it = 0; it < 2; ++it) {
                const u32x4 xf = *(const LAS u32x4*)(L + (16 * it + r16) * 1024 + (((4 * ks + q) & 48) | (((4 * ks + q) & 15) ^ r16)) * 16);
                sc[0][it] = mfma16(z0[ks], xf, sc[0][it]); sc[1][it] = mfma16(z1[ks], xf, sc[1][it]);
            }
        {
            const int row = tid >> 3, part = tid & 7;
            float g = 0;
#pragma unroll
            for (int k = 0; k < 7; ++k) {
                const int ch = part * 7 + k;
                const u32x4 a = *(const LAS u32x4*)(L + row * 1024 + ((ch & 48) | ((ch & 15) ^ (row & 15))) * 16), b = gld16(p.gw + ch * 8);
#pragma unroll
                for (int e = 0; e < 4; ++e) g += __uint_as_float(a[e] << 16) * __uint_as_float(b[e] << 16) + __uint_as_float(a[e] & 0xFFFF0000u) * __uint_as_float(b[e] & 0xFFFF0000u);
            }
            g += __shfl_xor(g, 1); g += __shfl_xor(g, 2); g += __shfl_xor(g, 4);
            if (part == 0) gate[row] = 1.0f / (1.0f + __expf(-g));
        }
        if (!wait_words<0>(rs, (int)OFF_FLAG_F + min(l, 27) * 4, s * 128, 0xC00 + s, false)) return;
        u32x4 fo[4];
#pragma unroll
        for (int k = 0; k < 4; ++k) { const int u = min(w + 4 * k, 13); fo[k] = bld16(rs, (int)OFF_FO + ((16 * (u & 1) + r16) * D + 112 * qi + 16 * (u >> 1) + 4 * q) * 4, s * (32 * D * 4)); }
        if (!wait_words<1>(rs, vmw, (s + 2) * 16384, 0x800 + s, late)) return;
        RST(0);
        u32x4 zm[14]; u32x2 yf[4][7];
#pragma unroll
        for (int ks = 0; ks < 14; ++ks) zm[ks] = bld16(rs, vzm + ks * 512, s * (16 * 14 * 512));
        SCHED();
#pragma unroll
        for (int ks = 0; ks < 14; ++ks)
#pragma unroll
            for (int it = 0; it < 2; ++it) {
                const u32x4 xf = *(const LAS u32x4*)(L + (16 * it + r16) * 1024 + (((4 * ks + q) & 48) | (((4 * ks + q) & 15) ^ r16)) * 16);
                sc[2][it] = mfma16(zm[ks], xf, sc[2][it]);
            }
#pragma unroll
        for (int it = 0; it < 2; ++it) {
            const int i = 16 * it + r16;
            float v[12];
#pragma unroll
            for (int r = 0; r < 4; ++r) {
                v[r] = (8 * q + r <= i) ? sc[0][it][r] * SC2 : -3.0e38f;
                v[4 + r] = (8 * q + 4 + r <= i) ? sc[1][it][r] * SC2 : -3.0e38f;
                v[8 + r] = q < 2 ? sc[2][it][r] * SC2 : -3.0e38f;
            }
            float mx = v[0];
#pragma unroll
            for (int k = 1; k < 12; ++k) mx = fmaxf(mx, v[k]);
            { const auto x = __builtin_amdgcn_permlane16_swap(__float_as_uint(mx), __float_as_uint(mx), false, false); mx = fmaxf(__uint_as_float(x[0]), __uint_as_float(x[1])); }
            { const auto x = __builtin_amdgcn_permlane32_swap(__float_as_uint(mx), __float_as_uint(mx), false, false); mx = fmaxf(__uint_as_float(x[0]), __uint_as_float(x[1])); }
            float sum = 0;
#pragma unroll
            for (int k = 0; k < 12; ++k) { v[k] = __builtin_amdgcn_exp2f(v[k] - mx); sum += v[k]; }
            { const auto x = __builtin_amdgcn_permlane16_swap(__float_as_uint(sum), __float_as_uint(sum), false, false); sum = __uint_as_float(x[0]) + __uint_as_float(x[1]); }
            { const auto x = __builtin_amdgcn_permlane32_swap(__float_as_uint(sum), __float_as_uint(sum), false, false); sum = __uint_as_float(x[0]) + __uint_as_float(x[1]); }
            const float rl = __builtin_amdgcn_rcpf(sum);
            const u32x4 px = {pk2(v[0] * rl, v[1] * rl), pk2(v[2] * rl, v[3] * rl), pk2(v[4] * rl, v[5] * rl), pk2(v[6] * rl, v[7] * rl)};
            bst16(px, rs, (int)OFF_PX + (h * 2 + it) * 1024 + l * 16, pb * (16 * 2 * 1024));
            const u32x2 pm = {pk2(v[8] * rl, v[9] * rl), pk2(v[10] * rl, v[11] * rl)};
            bst8(pm, rs, (int)OFF_PM + (h * 2 + it) * 512 + l * 8, pb * (16 * 2 * 512));
        }
        RST(1);
        SCHED();
#pragma unroll
        for (int hh = 0; hh < 4; ++hh)
#pragma unroll
            for (int t = 0; t < 7; ++t) yf[hh][t] = bld8(rs, q < 2 ? (int)OFF_U + (((4 * w + hh) * 5 + 4) * D + 112 * qi + 16 * t + r16) * 16 + q * 8 : OOB, s * (16 * 5 * D * 16));
        SCHED();
        asm volatile("s_waitcnt vmcnt(28)" ::: "memory");
        if (l == 0) bst4(1u, rs, (int)OFF_FLAG_P + (qi * 4 + w) * 4, s * 64);
        RST(2);
        if (!wait_words<0>(rs, (int)OFF_FLAG_P + (4 * w + (l & 3)) * 4, s * 64, 0xB00 + s, true)) return;
        RST(3);
        u32x4 px[4][2]; u32x2 pm[4][2];
#pragma unroll
        for (int hh = 0; hh < 4; ++hh)
#pragma unroll
            for (int it = 0; it < 2; ++it) {
                px[hh][it] = bld16(rs, (int)OFF_PX + ((4 * w + hh) * 2 + it) * 1024 + l * 16, pb * (16 * 2 * 1024));
                pm[hh][it] = bld8(rs, (int)OFF_PM + ((4 * w + hh) * 2 + it) * 512 + l * 8, pb * (16 * 2 * 512));
            }
        SCHED();
        f32x4 acc[7][2];
#pragma unroll
        for (int t = 0; t < 7; ++t) { acc[t][0] = f32x4{0, 0, 0, 0}; acc[t][1] = acc[t][0]; }
#pragma unroll
        for (int hh = 0; hh < 4; ++hh)
#pragma unroll
            for (int t = 0; t < 7; ++t)
#pragma unroll
                for (int it = 0; it < 2; ++it) { acc[t][it] = mfma16(uf[hh][t], px[hh][it], acc[t][it]); acc[t][it] = mfma16k16(yf[hh][t], pm[hh][it], acc[t][it]); }
#pragma unroll
        for (int t = 0; t < 7; ++t) { red[(w * 14 + 2 * t) * 64 + l] = acc[t][0]; red[(w * 14 + 2 * t + 1) * 64 + l] = acc[t][1]; }
        RST(4);
        lds_barrier();
#pragma unroll
        for (int k = 0; k < 4; ++k) {
            const int u = w + 4 * k;
            if (u < 14) {
                const f32x4 a = (red[(0 * 14 + u) * 64 + l] + red[(1 * 14 + u) * 64 + l]) + (red[(2 * 14 + u) * 64 + l] + red[(3 * 14 + u) * 64 + l]);
                const int i = 16 * (u & 1) + r16, f = 112 * qi + 16 * (u >> 1) + 4 * q;
                const float g = gate[i];
                const u32x4 bb = fo[k];
                const u32x2 o = {pk2(g * a[0] + (1.f - g) * __uint_as_float(bb[0]), g * a[1] + (1.f - g) * __uint_as_float(bb[1])), pk2(g * a[2] + (1.f - g) * __uint_as_float(bb[2]), g * a[3] + (1.f - g) * __uint_as_float(bb[3]))};
                *(GAS u32x2*)(p.out + ((size_t)s * 32 + i) * D + f) = o;
            }
        }
        RST(5);
    }
}

constexpr int CODE_WARM = 68 * 1024;
DI void warm_code(unsigned long long pc) {
    const unsigned char* base = (const unsigned char*)(pc & ~255ull);
    u32x4 t[CODE_WARM / 4096];
#pragma unroll
    for (int n = 0; n < CODE_WARM / 4096; ++n) t[n] = gld16(base + n * 4096 + threadIdx.x * 16);
#pragma unroll
    for (int n = 0; n < CODE_WARM / 4096; ++n) asm volatile("" :: "v"(t[n]));
}
__global__ void __launch_bounds__(256, 1) k_main(Params p) {
    extern __shared__ __attribute__((aligned(16))) unsigned char smem[];
    LAS unsigned char* L = (LAS unsigned char*)smem;
    const int b = blockIdx.x, xl = b & 7, idx = (b >> 3) * 4 + (xl & 3);
    if ((xl < 4 && idx >= 112 && idx < 120) || (xl >= 4 && idx >= 84 && idx < 92)) {
        unsigned long long pc; asm volatile("s_getpc_b64 %0" : "=s"(pc));
        warm_code(pc);
    }
    if (xl < 4) {
        if (idx < 112) chain_merged(p, idx, L); else chain_mx(p, idx - 112, L);
    } else if (!(p.pad & 1) || idx >= 84) {
        if (idx < 28) stream_x<0>(p, idx, L);
        else if (idx < 56) stream_x<1>(p, idx - 28, L);
        else if (idx < 70) { for (int i = 0; i < FFN_DELAY; ++i) __builtin_amdgcn_s_sleep(127); stream_x<2>(p, idx - 56, L); }
        else if (idx < 84) { for (int i = 0; i < FFN_DELAY; ++i) __builtin_amdgcn_s_sleep(127); ffn_down(p, idx - 70, L); }
        else if (!(p.pad & 3)) rows_role(p, idx - 84, L);
    }
}

#define TAU_M 900
#define TAU_S 850
#define TAU_X 2650
int g_tauM = TAU_M, g_tauS = TAU_S, g_tauX = TAU_X, g_dbg = 0;

extern "C" void kernel_launch(void* const* d_in, const int* in_sizes, int n_in, void* d_out, int out_size, void* d_ws, size_t ws_size, hipStream_t stream) {
    Params p;
    p.x = (const u16*)d_in[0]; p.mem0 = (const u16*)d_in[1]; p.Wq = (const u16*)d_in[2]; p.Wk = (const u16*)d_in[3]; p.Wv = (const u16*)d_in[4]; p.Wo = (const u16*)d_in[5];
    p.Wg = (const u16*)d_in[8]; p.Wu = (const u16*)d_in[9]; p.Wd = (const u16*)d_in[10]; p.gw = (const u16*)d_in[11];
    p.ws = (unsigned char*)(((uintptr_t)d_ws + 2 * MB - 1) & ~(uintptr_t)(2 * MB - 1));
    p.out = (u16*)d_out;
    p.tauM = g_tauM; p.tauS = g_tauS; p.tauX = 384; p.tauXm = g_tauX; p.pad = g_dbg; p.pad2 = 0;
    constexpr int LDS_MAIN = 2 * XB + 4 * 14 * 16 * 64;
    hipFuncSetAttribute((const void*)k_main, hipFuncAttributeMaxDynamicSharedMemorySize, LDS_MAIN);
    hipFuncSetAttribute((const void*)k_prep, hipFuncAttributeMaxDynamicSharedMemorySize, 65536);
    k_prep<<<NPREP, 256, 65536, stream>>>(p);
    k_main<<<256, 256, LDS_MAIN, stream>>>(p);

}
```
